```python
import torch

def _softmax(x, axis=-1):
    return torch.softmax(x, dim=axis)
def _gelu(x):
    return torch.nn.functional.gelu(x, approximate="tanh")
def _silu(x):
    return torch.nn.functional.silu(x)
def _sigmoid(x):
    return torch.sigmoid(x)
def _layernorm(x, axis=-1, eps=1e-5):
    m = x.mean(dim=axis, keepdim=True)
    v = x.var(dim=axis, keepdim=True, unbiased=False)
    return (x - m) / torch.sqrt(v + eps)
def _scatter_add(tgt, idx, src, axis=0):
    return tgt.clone().index_add_(axis, idx, src)
def _segment_sum(src, seg, n):
    out = torch.zeros((n,) + src.shape[1:], dtype=src.dtype)
    return out.index_add_(0, seg, src)
def _pad(x, pw):
    flat = tuple(v for pair in reversed(pw) for v in pair)
    return torch.nn.functional.pad(x, flat)

kernel_name = 'recurrent_scan__sliding_block__dense_activation_relu__prompt'

def setup_inputs():

    _w = (lambda a: (lambda t: t.to(torch.bfloat16) if t.is_floating_point() else t)(torch.as_tensor(a)))
    def _A_setup():
        B, S, D = 1, 2048, 640
        return {
            "x": _w((lambda *s: torch.randn(*s, dtype=torch.bfloat16) * 0.05)(B, S, D)),
            "wqkv": _w((lambda *s: torch.randn(*s, dtype=torch.bfloat16) * 0.05)(D, 3 * D)),
            "wo": _w((lambda *s: torch.randn(*s, dtype=torch.bfloat16) * 0.05)(D, D)),
        }
    _A_in = _A_setup()

    def _B_setup():
        B, S, D, Dff = 1, 2048, 640, 1536
        return {
            "x": _w((lambda *s: torch.randn(*s, dtype=torch.bfloat16) * 0.05)(B, S, D)),
            "w1": _w((lambda *s: torch.randn(*s, dtype=torch.bfloat16) * 0.05)(D, Dff)),
            "w2": _w((lambda *s: torch.randn(*s, dtype=torch.bfloat16) * 0.05)(Dff, D)),
        }
    _B_in = _B_setup()

    return {
        "A_x": _A_in["x"],
        "A_wqkv": _A_in["wqkv"],
        "A_wo": _A_in["wo"],
        "B_x": _B_in["x"],
        "B_w1": _B_in["w1"],
        "B_w2": _B_in["w2"],
    }

def reference(A_x, A_wqkv, A_wo, B_x, B_w1, B_w2):
    def _A_ref(x, wqkv, wo):
        B, S, D = x.shape
        H = 32; Dh = D // H; W = 256
        qkv = (lambda x, *d: x.permute(*d))((x @ wqkv).reshape(B, S, 3, H, Dh), 2, 0, 3, 1, 4)
        q, k, v = qkv[0], qkv[1], qkv[2]
        scores = torch.einsum("bhqd,bhkd->bhqk", q, k) * (Dh ** -0.5)
        idx = torch.arange(S)
        mask = (idx[:, None] - idx[None, :] >= 0) & (idx[:, None] - idx[None, :] < W)
        scores = torch.where(mask[None, None], scores, -1e9)
        p = _softmax(scores, axis=-1)
        o = torch.einsum("bhqk,bhkd->bhqd", p, v)
        return (lambda x, *d: x.permute(*d))(o, 0, 2, 1, 3).reshape(B, S, D) @ wo

    def _B_ref(x, w1, w2):
        h = torch.einsum("bsd,df->bsf", x, w1)
        zero = torch.zeros_like(h)
        h = torch.maximum(h, zero)
        return torch.einsum("bsf,fd->bsd", h, w2)

    x = A_x
    xn = _layernorm(x, axis=-1)
    s = _A_ref(xn, A_wqkv, A_wo)
    g = _silu(_B_ref(xn, B_w1, B_w2))
    return x + s * g
```

```cpp
#include <hip/hip_runtime.h>
#include <hip/hip_bf16.h>
#include <stdint.h>

typedef short bf16x8 __attribute__((ext_vector_type(8)));
typedef short s16x4 __attribute__((ext_vector_type(4)));
typedef __bf16 bf16x2_t __attribute__((ext_vector_type(2)));
typedef __bf16 bf16x4_t __attribute__((ext_vector_type(4)));
typedef float f32x16 __attribute__((ext_vector_type(16)));
typedef float f32x4 __attribute__((ext_vector_type(4)));
typedef unsigned u32x4 __attribute__((ext_vector_type(4)));
typedef unsigned u32x2 __attribute__((ext_vector_type(2)));
typedef unsigned short u16;

#define DI __device__ __forceinline__
#define MFMA32(a, b, c) __builtin_amdgcn_mfma_f32_32x32x16_bf16((a), (b), (c), 0, 0, 0)

constexpr int S_ = 2048, D_ = 640, FF_ = 1536, NH_ = 32, DH_ = 20, WIN_ = 256;

DI void wait_vm(int n) {
  if (n == 0) asm volatile("s_waitcnt vmcnt(0)" ::: "memory");
  else if (n == 2) asm volatile("s_waitcnt vmcnt(2)" ::: "memory");
  else if (n == 3) asm volatile("s_waitcnt vmcnt(3)" ::: "memory");
  else if (n == 4) asm volatile("s_waitcnt vmcnt(4)" ::: "memory");
  else if (n == 6) asm volatile("s_waitcnt vmcnt(6)" ::: "memory");
  else if (n == 8) asm volatile("s_waitcnt vmcnt(8)" ::: "memory");
  else asm volatile("s_waitcnt vmcnt(0)" ::: "memory");
}
DI void bar() {
  asm volatile("s_waitcnt lgkmcnt(0)" ::: "memory");
  __builtin_amdgcn_s_barrier();
  asm volatile("" ::: "memory");
}
DI u16 f2bf(float f) { return __builtin_bit_cast(u16, (__bf16)f); }
DI float bf2f(u16 v) { return __builtin_bit_cast(float, (unsigned)v << 16); }
DI float rbf(float f) { return bf2f(f2bf(f)); }
DI unsigned pk2(float a, float b) { return (unsigned)f2bf(a) | ((unsigned)f2bf(b) << 16); }
DI float dot2acc(unsigned a, unsigned b, float c) {
  return __builtin_amdgcn_fdot2_f32_bf16(__builtin_bit_cast(bf16x2_t, a), __builtin_bit_cast(bf16x2_t, b), c, false);
}
DI s16x4 tr_read(const char* p) {
  bf16x4_t v = __builtin_amdgcn_ds_read_tr16_b64_v4bf16((__attribute__((address_space(3))) bf16x4_t*)(p));
  return __builtin_bit_cast(s16x4, v);
}
DI void glds16(const void* g, char* l) {
  __builtin_amdgcn_global_load_lds((const __attribute__((address_space(1))) void*)g,
                                   (__attribute__((address_space(3))) void*)l, 16, 0, 0);
}

template <int BM, int BN, int WM, int WN, bool SWAP, bool STATS>
struct Gemm {
  static constexpr int MT = BM / WM / 32, NT = BN / WN / 32;
  static constexpr int A_BYTES = BM * 128, B_BYTES = 64 * BN * 2, STAGE_BYTES = A_BYTES + B_BYTES;
  static constexpr int A_GLDS = BM * 8 / 256, B_CPR = BN / 8, B_GLDS = 64 * B_CPR / 256;
  static constexpr int NG = A_GLDS + B_GLDS;
  static constexpr int LDS_BYTES = 2 * STAGE_BYTES;

  DI static int swzB(int k) { return (B_CPR == 16) ? ((k & 3) << 2) : (((k >> 1) & 1) << 2); }

  DI static void stage(char* buf, const u16* __restrict__ A, int lda, const u16* __restrict__ B, int ldb, int k0, int tid) {
    const int wbase = (tid & ~63) * 16;
#pragma unroll
    for (int i = 0; i < A_GLDS; ++i) {
      const int ci = i * 256 + tid, row = ci >> 3, c = (ci & 7) ^ ((row >> 1) & 7);
      glds16(A + (size_t)row * lda + k0 + c * 8, buf + i * 4096 + wbase);
    }
#pragma unroll
    for (int i = 0; i < B_GLDS; ++i) {
      const int ci = i * 256 + tid, k = ci / B_CPR, c = (ci % B_CPR) ^ swzB(k);
      glds16(B + (size_t)(k0 + k) * ldb + c * 8, buf + A_BYTES + i * 4096 + wbase);
    }
  }

  DI static void run(char* lds, const u16* __restrict__ A, int lda, const u16* __restrict__ B, int ldb, int nk,
                     f32x16 (&acc)[MT][NT], float (&rsum)[MT], float (&rsq)[MT], float (&csum)[NT]) {
    const int tid = threadIdx.x, lane = tid & 63, w = tid >> 6, wm = w / WN, wn = w % WN;
    const int r = lane & 31, hh = lane >> 5;
    const int blk = (lane >> 4) & 1, q = (lane & 15) >> 2, p = lane & 3;
#pragma unroll
    for (int mi = 0; mi < MT; ++mi) {
      rsum[mi] = 0.f; rsq[mi] = 0.f;
#pragma unroll
      for (int ni = 0; ni < NT; ++ni)
#pragma unroll
        for (int i = 0; i < 16; ++i) acc[mi][ni][i] = 0.f;
    }
#pragma unroll
    for (int ni = 0; ni < NT; ++ni) csum[ni] = 0.f;
    const unsigned ONES = 0x3f803f80u;

    stage(lds, A, lda, B, ldb, 0, tid);
    for (int kt = 0; kt < nk; ++kt) {
      char* cur = lds + (kt & 1) * STAGE_BYTES;
      if (kt + 1 < nk) { stage(lds + ((kt + 1) & 1) * STAGE_BYTES, A, lda, B, ldb, (kt + 1) * 64, tid); wait_vm(NG); }
      else wait_vm(0);
      bar();
      const char* As = cur;
      const char* Bs = cur + A_BYTES;
#pragma unroll
      for (int s = 0; s < 4; ++s) {
        bf16x8 af[MT], bfr[NT];
#pragma unroll
        for (int mi = 0; mi < MT; ++mi) {
          const int row = (wm * MT + mi) * 32 + r, c = 2 * s + hh;
          af[mi] = *(const bf16x8*)(As + row * 128 + ((c ^ ((row >> 1) & 7)) << 4));
        }
#pragma unroll
        for (int ni = 0; ni < NT; ++ni) {
          const int cc = ((wn * NT + ni) * 32 + blk * 16 + 4 * p) >> 3;
          const int k1 = 16 * s + 8 * hh + q, k2 = k1 + 4;
          s16x4 lo = tr_read(Bs + k1 * (BN * 2) + ((cc ^ swzB(k1)) << 4) + (p & 1) * 8);
          s16x4 hi = tr_read(Bs + k2 * (BN * 2) + ((cc ^ swzB(k2)) << 4) + (p & 1) * 8);
          bfr[ni] = __builtin_shufflevector(lo, hi, 0, 1, 2, 3, 4, 5, 6, 7);
        }
        if (STATS) {
#pragma unroll
          for (int mi = 0; mi < MT; ++mi) {
            u32x4 u = __builtin_bit_cast(u32x4, af[mi]);
#pragma unroll
            for (int j = 0; j < 4; ++j) { rsum[mi] = dot2acc(u[j], ONES, rsum[mi]); rsq[mi] = dot2acc(u[j], u[j], rsq[mi]); }
          }
#pragma unroll
          for (int ni = 0; ni < NT; ++ni) {
            u32x4 u = __builtin_bit_cast(u32x4, bfr[ni]);
#pragma unroll
            for (int j = 0; j < 4; ++j) csum[ni] = dot2acc(u[j], ONES, csum[ni]);
          }
        }
#pragma unroll
        for (int mi = 0; mi < MT; ++mi)
#pragma unroll
          for (int ni = 0; ni < NT; ++ni)
            acc[mi][ni] = SWAP ? MFMA32(bfr[ni], af[mi], acc[mi][ni]) : MFMA32(af[mi], bfr[ni], acc[mi][ni]);
      }
      bar();
    }
  }
};

typedef Gemm<128, 128, 2, 2, true, true> G1s;
typedef Gemm<128, 128, 2, 2, false, true> G1n;

__global__ __launch_bounds__(256, 2) void k1_qkv_h(const u16* __restrict__ x, const u16* __restrict__ wqkv, const u16* __restrict__ w1,
                                                   u16* __restrict__ qb, u16* __restrict__ kb, u16* __restrict__ vT, u16* __restrict__ hb) {
  __shared__ __attribute__((aligned(16))) char lds[G1s::LDS_BYTES];
  const int nt = blockIdx.x % 27, mt = blockIdx.x / 27;
  const int tid = threadIdx.x, lane = tid & 63, w = tid >> 6, wm = w >> 1, wn = w & 1, r = lane & 31, hh = lane >> 5;
  const u16* A = x + (size_t)mt * 128 * D_;
  const u16* B; int ldb;
  if (nt < 15) { B = wqkv + nt * 128; ldb = 3 * D_; } else { B = w1 + (nt - 15) * 128; ldb = FF_; }
  f32x16 acc[2][2]; float rsum[2], rsq[2], csum[2];
  const bool isV = (nt >= 10 && nt < 15);
  if (isV) G1n::run(lds, A, D_, B, ldb, D_ / 64, acc, rsum, rsq, csum);
  else     G1s::run(lds, A, D_, B, ldb, D_ / 64, acc, rsum, rsq, csum);
  float mean[2], rstd[2];
#pragma unroll
  for (int mi = 0; mi < 2; ++mi) {
    float s1 = rsum[mi] + __shfl_xor(rsum[mi], 32), s2 = rsq[mi] + __shfl_xor(rsq[mi], 32);
    float m = s1 * (1.f / D_), v = fmaxf(s2 * (1.f / D_) - m * m, 0.f);
    mean[mi] = m; rstd[mi] = rsqrtf(v + 1e-5f);
  }
#pragma unroll
  for (int ni = 0; ni < 2; ++ni) csum[ni] += __shfl_xor(csum[ni], 32);
  float* sc = (float*)(lds) + w * 256;
  if (hh == 0) {
#pragma unroll
    for (int i = 0; i < 2; ++i) { sc[i * 32 + r] = csum[i]; sc[64 + i * 32 + r] = mean[i]; sc[128 + i * 32 + r] = rstd[i]; }
  }
  asm volatile("s_waitcnt lgkmcnt(0)" ::: "memory");
  __builtin_amdgcn_wave_barrier();
  const int tok0 = mt * 128 + wm * 64, n0 = wn * 64;
  if (!isV) {
    u16* dst; int ldd, cbase; bool relu = false;
    if (nt < 5) { dst = qb; ldd = D_; cbase = nt * 128; }
    else if (nt < 10) { dst = kb; ldd = D_; cbase = (nt - 5) * 128; }
    else { dst = hb; ldd = FF_; cbase = (nt - 15) * 128; relu = true; }
#pragma unroll
    for (int ni = 0; ni < 2; ++ni) {
      f32x4 cs[4];
#pragma unroll
      for (int qd = 0; qd < 4; ++qd) cs[qd] = *(const f32x4*)(sc + ni * 32 + 8 * qd + 4 * hh);
#pragma unroll
      for (int mi = 0; mi < 2; ++mi) {
        u16* row = dst + (size_t)(tok0 + mi * 32 + r) * ldd + cbase + n0 + ni * 32 + 4 * hh;
#pragma unroll
        for (int qd = 0; qd < 4; ++qd) {
          float v[4];
#pragma unroll
          for (int j = 0; j < 4; ++j) {
            float t = rstd[mi] * (acc[mi][ni][4 * qd + j] - mean[mi] * cs[qd][j]);
            v[j] = relu ? fmaxf(t, 0.f) : t;
          }
          u32x2 o = {pk2(v[0], v[1]), pk2(v[2], v[3])};
          *(u32x2*)(row + 8 * qd) = o;
        }
      }
    }
  } else {
    const int cbase = (nt - 10) * 128;
#pragma unroll
    for (int mi = 0; mi < 2; ++mi) {
      f32x4 mn[4], rs[4];
#pragma unroll
      for (int qd = 0; qd < 4; ++qd) {
        mn[qd] = *(const f32x4*)(sc + 64 + mi * 32 + 8 * qd + 4 * hh);
        rs[qd] = *(const f32x4*)(sc + 128 + mi * 32 + 8 * qd + 4 * hh);
      }
#pragma unroll
      for (int ni = 0; ni < 2; ++ni) {
        u16* row = vT + (size_t)(cbase + n0 + ni * 32 + r) * S_ + tok0 + mi * 32 + 4 * hh;
#pragma unroll
        for (int qd = 0; qd < 4; ++qd) {
          float v[4];
#pragma unroll
          for (int j = 0; j < 4; ++j) v[j] = rs[qd][j] * (acc[mi][ni][4 * qd + j] - mn[qd][j] * csum[ni]);
          u32x2 o = {pk2(v[0], v[1]), pk2(v[2], v[3])};
          *(u32x2*)(row + 8 * qd) = o;
        }
      }
    }
  }
}

typedef Gemm<128, 64, 2, 2, true, false> G2;
constexpr int G2_BLOCKS = (S_ / 128) * (D_ / 64);
constexpr int ATT_BLOCKS = NH_ * (S_ / 128);
constexpr int KS_BYTES = 384 * 64, VS_STRIDE = 776;

__global__ __launch_bounds__(256, 2) void k2_attn_w2(const u16* __restrict__ qb, const u16* __restrict__ kb, const u16* __restrict__ vT,
                                                     const u16* __restrict__ hb, const u16* __restrict__ w2,
                                                     u16* __restrict__ ob, u16* __restrict__ gb) {
  __shared__ __attribute__((aligned(16))) char lds[G2::LDS_BYTES];
  const int tid = threadIdx.x, lane = tid & 63, w = tid >> 6, r = lane & 31, hh = lane >> 5;
  if (blockIdx.x < G2_BLOCKS) {
    const int nt = blockIdx.x % (D_ / 64), mt = blockIdx.x / (D_ / 64);
    f32x16 acc[2][1]; float d0[2], d1[2], d2[1];
    G2::run(lds, hb + (size_t)mt * 128 * FF_, FF_, w2 + nt * 64, D_, FF_ / 64, acc, d0, d1, d2);
    const int wm = w >> 1, wn = w & 1;
#pragma unroll
    for (int mi = 0; mi < 2; ++mi) {
      u16* row = gb + (size_t)(mt * 128 + wm * 64 + mi * 32 + r) * D_ + nt * 64 + wn * 32 + 4 * hh;
#pragma unroll
      for (int qd = 0; qd < 4; ++qd) {
        u32x2 o = {pk2(acc[mi][0][4 * qd], acc[mi][0][4 * qd + 1]), pk2(acc[mi][0][4 * qd + 2], acc[mi][0][4 * qd + 3])};
        *(u32x2*)(row + 8 * qd) = o;
      }
    }
    return;
  }
  const int ab = blockIdx.x - G2_BLOCKS, h = ab % NH_, q0 = (ab / NH_) * 128;
  const int kstart = q0 - WIN_;
  char* Ks = lds;
  char* Vs = lds + KS_BYTES;
  for (int ki = tid; ki < 384; ki += 256) {
    const int key = kstart + ki;
    u32x2 a0 = {0, 0}, a1 = {0, 0}, a2 = {0, 0}, a3 = {0, 0}, a4 = {0, 0};
    if (key >= 0) {
      const u32x2* src = (const u32x2*)(kb + (size_t)key * D_ + h * DH_);
      a0 = src[0]; a1 = src[1]; a2 = src[2]; a3 = src[3]; a4 = src[4];
    }
    const int sw = (ki >> 2) & 3;
    char* rowp = Ks + ki * 64;
    *(u32x4*)(rowp + ((0 ^ sw) << 4)) = (u32x4){a0[0], a0[1], a1[0], a1[1]};
    *(u32x4*)(rowp + ((1 ^ sw) << 4)) = (u32x4){a2[0], a2[1], a3[0], a3[1]};
    *(u32x4*)(rowp + ((2 ^ sw) << 4)) = (u32x4){a4[0], a4[1], 0u, 0u};
    *(u32x4*)(rowp + ((3 ^ sw) << 4)) = (u32x4){0u, 0u, 0u, 0u};
  }
  for (int idx = tid; idx < DH_ * 48; idx += 256) {
    const int d = idx / 48, c = idx % 48, key0 = kstart + c * 8;
    u32x4 v = {0, 0, 0, 0};
    if (key0 >= 0) v = *(const u32x4*)(vT + (size_t)(h * DH_ + d) * S_ + key0);
    char* dst = Vs + d * VS_STRIDE + c * 16;
    *(u32x2*)(dst) = (u32x2){v[0], v[1]};
    *(u32x2*)(dst + 8) = (u32x2){v[2], v[3]};
  }
  const int qw = q0 + 32 * w;
  bf16x8 qf0, qf1;
  {
    const u32x2* src = (const u32x2*)(qb + (size_t)(qw + r) * D_ + h * DH_);
    u32x2 a = src[2 * hh], b = src[2 * hh + 1], c = {0, 0};
    if (hh == 0) c = src[4];
    u32x4 f0 = {a[0], a[1], b[0], b[1]}, f1 = {c[0], c[1], 0u, 0u};
    qf0 = __builtin_bit_cast(bf16x8, f0); qf1 = __builtin_bit_cast(bf16x8, f1);
  }
  __syncthreads();
  const float NEG = -__builtin_inff();
  f32x16 sc[9];
#pragma unroll
  for (int t = 0; t < 9; ++t) {
    const int kbase = qw - WIN_ + 32 * t;
    if (kbase >= 0) {
      const int row = 32 * (w + t) + r, sw = (row >> 2) & 3;
      bf16x8 k0 = *(const bf16x8*)(Ks + row * 64 + ((hh ^ sw) << 4));
      bf16x8 k1 = *(const bf16x8*)(Ks + row * 64 + (((2 + hh) ^ sw) << 4));
      f32x16 z;
#pragma unroll
      for (int i = 0; i < 16; ++i) z[i] = 0.f;
      z = MFMA32(k0, qf0, z);
      z = MFMA32(k1, qf1, z);
      sc[t] = z;
    } else {
#pragma unroll
      for (int i = 0; i < 16; ++i) sc[t][i] = NEG;
    }
  }
#pragma unroll
  for (int i = 0; i < 16; ++i) {
    const int kr = (i & 3) + 8 * (i >> 2) + 4 * hh;
    if (kr <= r) sc[0][i] = NEG;
    if (kr > r) sc[8][i] = NEG;
  }
  float mx = NEG;
#pragma unroll
  for (int t = 0; t < 9; ++t)
#pragma unroll
    for (int i = 0; i < 16; ++i) mx = fmaxf(mx, sc[t][i]);
  mx = fmaxf(mx, __shfl_xor(mx, 32));
  const float C2 = 0.22360679774997896f * 1.4426950408889634f;
  const float mo = -mx * C2;
  float sum = 0.f;
  f32x16 oacc;
#pragma unroll
  for (int i = 0; i < 16; ++i) oacc[i] = 0.f;
  const bool dval = r < DH_;
  const char* vrow = Vs + (dval ? r : 0) * VS_STRIDE;
#pragma unroll
  for (int t = 0; t < 9; ++t) {
    const int kbase = qw - WIN_ + 32 * t;
    if (kbase >= 0) {
      float pv[16];
#pragma unroll
      for (int i = 0; i < 16; ++i) { pv[i] = __builtin_amdgcn_exp2f(fmaf(sc[t][i], C2, mo)); sum += pv[i]; }
#pragma unroll
      for (int s = 0; s < 2; ++s) {
        u32x4 pp = {pk2(pv[8 * s], pv[8 * s + 1]), pk2(pv[8 * s + 2], pv[8 * s + 3]), pk2(pv[8 * s + 4], pv[8 * s + 5]), pk2(pv[8 * s + 6], pv[8 * s + 7])};
        const int kidx = 32 * (w + t) + 16 * s + 4 * hh;
        u32x2 v0 = *(const u32x2*)(vrow + kidx * 2);
        u32x2 v1 = *(const u32x2*)(vrow + kidx * 2 + 16);
        u32x4 vv = {v0[0], v0[1], v1[0], v1[1]};
        if (!dval) vv = (u32x4){0u, 0u, 0u, 0u};
        oacc = MFMA32(__builtin_bit_cast(bf16x8, vv), __builtin_bit_cast(bf16x8, pp), oacc);
      }
    }
  }
  sum += __shfl_xor(sum, 32);
  const float inv = 1.f / sum;
  u16* orow = ob + (size_t)(qw + r) * D_ + h * DH_ + 4 * hh;
#pragma unroll
  for (int qd = 0; qd < 3; ++qd) {
    if (qd < 2 || hh == 0) {
      u32x2 o = {pk2(oacc[4 * qd] * inv, oacc[4 * qd + 1] * inv), pk2(oacc[4 * qd + 2] * inv, oacc[4 * qd + 3] * inv)};
      *(u32x2*)(orow + 8 * qd) = o;
    }
  }
}

typedef Gemm<64, 64, 2, 2, true, false> G3;

__global__ __launch_bounds__(256, 2) void k3_out(const u16* __restrict__ ob, const u16* __restrict__ wo, const u16* __restrict__ gb,
                                                 const u16* __restrict__ x, u16* __restrict__ out) {
  __shared__ __attribute__((aligned(16))) char lds[G3::LDS_BYTES];
  const int tid = threadIdx.x, lane = tid & 63, w = tid >> 6, r = lane & 31, hh = lane >> 5, wm = w >> 1, wn = w & 1;
  const int nt = blockIdx.x % (D_ / 64), mt = blockIdx.x / (D_ / 64);
  f32x16 acc[1][1]; float d0[1], d1[1], d2[1];
  G3::run(lds, ob + (size_t)mt * 64 * D_, D_, wo + nt * 64, D_, D_ / 64, acc, d0, d1, d2);
  const size_t off = (size_t)(mt * 64 + wm * 32 + r) * D_ + nt * 64 + wn * 32 + 4 * hh;
#pragma unroll
  for (int qd = 0; qd < 4; ++qd) {
    u32x2 gv = *(const u32x2*)(gb + off + 8 * qd);
    u32x2 xv = *(const u32x2*)(x + off + 8 * qd);
    float o[4];
#pragma unroll
    for (int j = 0; j < 4; ++j) {
      const float g = bf2f((u16)(gv[j >> 1] >> (16 * (j & 1))));
      const float xx = bf2f((u16)(xv[j >> 1] >> (16 * (j & 1))));
      const float sg = rbf(g / (1.f + __expf(-g)));
      const float pr = rbf(rbf(acc[0][0][4 * qd + j]) * sg);
      o[j] = xx + pr;
    }
    u32x2 ov = {pk2(o[0], o[1]), pk2(o[2], o[3])};
    *(u32x2*)(out + off + 8 * qd) = ov;
  }
}

extern "C" void kernel_launch(void* const* d_in, const int* in_sizes, int n_in,
                              void* d_out, int out_size, void* d_ws, size_t ws_size,
                              hipStream_t stream) {
  const u16* x = (const u16*)d_in[0];
  const u16* wqkv = (const u16*)d_in[1];
  const u16* wo = (const u16*)d_in[2];
  const u16* w1 = (const u16*)d_in[4];
  const u16* w2 = (const u16*)d_in[5];
  u16* out = (u16*)d_out;
  char* ws = (char*)d_ws;
  const size_t SD = (size_t)S_ * D_ * 2;
  u16* qb = (u16*)(ws);
  u16* kb = (u16*)(ws + SD);
  u16* vT = (u16*)(ws + 2 * SD);
  u16* ob = (u16*)(ws + 3 * SD);
  u16* gb = (u16*)(ws + 4 * SD);
  u16* hb = (u16*)(ws + 5 * SD);
  k1_qkv_h<<<16 * 27, 256, 0, stream>>>(x, wqkv, w1, qb, kb, vT, hb);
  k2_attn_w2<<<G2_BLOCKS + ATT_BLOCKS, 256, 0, stream>>>(qb, kb, vT, hb, w2, ob, gb);
  k3_out<<<(S_ / 64) * (D_ / 64), 256, 0, stream>>>(ob, wo, gb, x, out);
}
```

```cpp
#include <hip/hip_runtime.h>
#include <hip/hip_bf16.h>
#include <stdint.h>

typedef short bf16x8 __attribute__((ext_vector_type(8)));
typedef short s16x4 __attribute__((ext_vector_type(4)));
typedef __bf16 bf16x2_t __attribute__((ext_vector_type(2)));
typedef __bf16 bf16x4_t __attribute__((ext_vector_type(4)));
typedef float f32x16 __attribute__((ext_vector_type(16)));
typedef float f32x4 __attribute__((ext_vector_type(4)));
typedef unsigned u32x4 __attribute__((ext_vector_type(4)));
typedef unsigned u32x2 __attribute__((ext_vector_type(2)));
typedef unsigned short u16;

#define DI __device__ __forceinline__
#define MFMA32(a, b, c) __builtin_amdgcn_mfma_f32_32x32x16_bf16((a), (b), (c), 0, 0, 0)

#define STAMP(i) do { } while (0)

constexpr int S_ = 2048, D_ = 640, FF_ = 1536, NH_ = 32, DH_ = 20, WIN_ = 256;

template <int N> DI void wait_vm_t() { asm volatile("s_waitcnt vmcnt(%0)" ::"n"(N) : "memory"); }
DI void wait_vm(int n) {
  switch (n) {
    case 0: wait_vm_t<0>(); break;   case 2: wait_vm_t<2>(); break;   case 3: wait_vm_t<3>(); break;
    case 4: wait_vm_t<4>(); break;   case 6: wait_vm_t<6>(); break;   case 8: wait_vm_t<8>(); break;
    case 9: wait_vm_t<9>(); break;   case 12: wait_vm_t<12>(); break; case 16: wait_vm_t<16>(); break;
    case 18: wait_vm_t<18>(); break; case 24: wait_vm_t<24>(); break;
    default: wait_vm_t<0>(); break;
  }
}
DI void bar() {
  asm volatile("s_waitcnt lgkmcnt(0)" ::: "memory");
  __builtin_amdgcn_s_barrier();
  asm volatile("" ::: "memory");
}
DI void wave_lds_sync() { asm volatile("s_waitcnt lgkmcnt(0)" ::: "memory"); __builtin_amdgcn_wave_barrier(); }
DI u16 f2bf(float f) { return __builtin_bit_cast(u16, (__bf16)f); }
DI float bf2f(u16 v) { return __builtin_bit_cast(float, (unsigned)v << 16); }
DI float rbf(float f) { return bf2f(f2bf(f)); }
DI unsigned pk2(float a, float b) { return (unsigned)f2bf(a) | ((unsigned)f2bf(b) << 16); }
DI float dot2acc(unsigned a, unsigned b, float c) {
  return __builtin_amdgcn_fdot2_f32_bf16(__builtin_bit_cast(bf16x2_t, a), __builtin_bit_cast(bf16x2_t, b), c, false);
}
DI s16x4 tr_read(const char* p) {
  bf16x4_t v = __builtin_amdgcn_ds_read_tr16_b64_v4bf16((__attribute__((address_space(3))) bf16x4_t*)(p));
  return __builtin_bit_cast(s16x4, v);
}
DI void glds16(const void* g, char* l) {
  __builtin_amdgcn_global_load_lds((const __attribute__((address_space(1))) void*)g,
                                   (__attribute__((address_space(3))) void*)l, 16, 0, 0);
}

template <int BM, int BN, int WM, int WN, int NS, int NK, bool SWAP, bool STATS>
struct Gemm {
  static constexpr int NTH = WM * WN * 64;
  static constexpr int MT = BM / WM / 32, NT = BN / WN / 32;
  static constexpr int A_BYTES = BM * 128, B_BYTES = 64 * BN * 2, STAGE_BYTES = A_BYTES + B_BYTES;
  static constexpr int A_GLDS = BM * 8 / NTH, B_CPR = BN / 8, B_GLDS = 64 * B_CPR / NTH;
  static constexpr int NG = A_GLDS + B_GLDS;
  static constexpr int LDS_BYTES = NS * STAGE_BYTES;

  DI static int swzB(int k) { return (B_CPR == 16) ? ((k & 3) << 2) : (((k >> 1) & 1) << 2); }

  DI static void init_ptrs(const u16* __restrict__ A, int lda, const u16* __restrict__ B, int ldb, int tid,
                           const u16* (&pa)[A_GLDS], const u16* (&pb)[B_GLDS]) {
#pragma unroll
    for (int i = 0; i < A_GLDS; ++i) {
      const int ci = i * NTH + tid, row = ci >> 3, c = (ci & 7) ^ ((row >> 1) & 7);
      pa[i] = A + (size_t)row * lda + c * 8;
    }
#pragma unroll
    for (int i = 0; i < B_GLDS; ++i) {
      const int ci = i * NTH + tid, k = ci / B_CPR, c = (ci % B_CPR) ^ swzB(k);
      pb[i] = B + (size_t)k * ldb + c * 8;
    }
  }
  DI static void stage(char* buf, const u16* (&pa)[A_GLDS], const u16* (&pb)[B_GLDS], int ldb, int wbase) {
#pragma unroll
    for (int i = 0; i < A_GLDS; ++i) {
      glds16(pa[i], buf + i * (NTH * 16) + wbase);
      pa[i] += 64; asm volatile("" : "+v"(pa[i]));
    }
#pragma unroll
    for (int i = 0; i < B_GLDS; ++i) {
      glds16(pb[i], buf + A_BYTES + i * (NTH * 16) + wbase);
      pb[i] += (size_t)64 * ldb; asm volatile("" : "+v"(pb[i]));
    }
  }

  DI static void run(char* lds, const u16* __restrict__ A, int lda, const u16* __restrict__ B, int ldb,
                     f32x16 (&acc)[MT][NT], float& rsum, float& rsq, float (&csum)[NT]) {
    const int tid = threadIdx.x, lane = tid & 63;
    const int w = __builtin_amdgcn_readfirstlane(tid >> 6), wm = w / WN, wn = w % WN;
    const int r = lane & 31, hh = lane >> 5;
    const int blk = (lane >> 4) & 1, q = (lane & 15) >> 2, p = lane & 3;
#pragma unroll
    for (int mi = 0; mi < MT; ++mi)
#pragma unroll
      for (int ni = 0; ni < NT; ++ni)
#pragma unroll
        for (int i = 0; i < 16; ++i) acc[mi][ni][i] = 0.f;
    rsum = 0.f; rsq = 0.f;
#pragma unroll
    for (int ni = 0; ni < NT; ++ni) csum[ni] = 0.f;
    const unsigned ONES = 0x3f803f80u;

    const u16* pa[A_GLDS]; const u16* pb[B_GLDS];
    init_ptrs(A, lda, B, ldb, tid, pa, pb);
    const int wbase = w * 1024;
#pragma unroll
    for (int kt = 0; kt < NS - 1; ++kt) stage(lds + kt * STAGE_BYTES, pa, pb, ldb, wbase);
#pragma unroll
    for (int kt = 0; kt < NK; ++kt) {
      char* cur = lds + (kt % NS) * STAGE_BYTES;
      {
        const int last = (kt + NS - 2 < NK - 1) ? (kt + NS - 2) : (NK - 1);
        wait_vm((last - kt) * NG);
      }
      bar();
      if (kt + NS - 1 < NK) stage(lds + ((kt + NS - 1) % NS) * STAGE_BYTES, pa, pb, ldb, wbase);
      const char* As = cur;
      const char* Bs = cur + A_BYTES;
#pragma unroll
      for (int s = 0; s < 4; ++s) {
        bf16x8 af[MT], bfr[NT];
#pragma unroll
        for (int mi = 0; mi < MT; ++mi) {
          const int row = (wm * MT + mi) * 32 + r, c = 2 * s + hh;
          af[mi] = *(const bf16x8*)(As + row * 128 + ((c ^ ((row >> 1) & 7)) << 4));
        }
#pragma unroll
        for (int ni = 0; ni < NT; ++ni) {
          const int cc = ((wn * NT + ni) * 32 + blk * 16 + 4 * p) >> 3;
          const int k1 = 16 * s + 8 * hh + q, k2 = k1 + 4;
          s16x4 lo = tr_read(Bs + k1 * (BN * 2) + ((cc ^ swzB(k1)) << 4) + (p & 1) * 8);
          s16x4 hi = tr_read(Bs + k2 * (BN * 2) + ((cc ^ swzB(k2)) << 4) + (p & 1) * 8);
          bfr[ni] = __builtin_shufflevector(lo, hi, 0, 1, 2, 3, 4, 5, 6, 7);
        }
        if (STATS) {
          u32x4 u = __builtin_bit_cast(u32x4, af[0]);
#pragma unroll
          for (int mi = 1; mi < MT; ++mi) if (wn == mi) u = __builtin_bit_cast(u32x4, af[mi]);
#pragma unroll
          for (int j = 0; j < 4; ++j) { rsum = dot2acc(u[j], ONES, rsum); rsq = dot2acc(u[j], u[j], rsq); }
        }
#pragma unroll
        for (int mi = 0; mi < MT; ++mi)
#pragma unroll
          for (int ni = 0; ni < NT; ++ni)
            acc[mi][ni] = SWAP ? MFMA32(bfr[ni], af[mi], acc[mi][ni]) : MFMA32(af[mi], bfr[ni], acc[mi][ni]);
      }
      if (STATS) {
#pragma unroll
        for (int ni = 0; ni < NT; ++ni) {
          const int cc = ((wn * NT + ni) * 32 + blk * 16 + 4 * p) >> 3;
          const int k1 = 16 * wm + 8 * hh + q, k2 = k1 + 4;
          s16x4 lo = tr_read(Bs + k1 * (BN * 2) + ((cc ^ swzB(k1)) << 4) + (p & 1) * 8);
          s16x4 hi = tr_read(Bs + k2 * (BN * 2) + ((cc ^ swzB(k2)) << 4) + (p & 1) * 8);
          u32x2 u0 = __builtin_bit_cast(u32x2, lo), u1 = __builtin_bit_cast(u32x2, hi);
          csum[ni] = dot2acc(u0[0], ONES, csum[ni]); csum[ni] = dot2acc(u0[1], ONES, csum[ni]);
          csum[ni] = dot2acc(u1[0], ONES, csum[ni]); csum[ni] = dot2acc(u1[1], ONES, csum[ni]);
        }
      }
    }
    bar();
  }
};

typedef Gemm<256, 128, 4, 2, 3, 10, true, true> G1s;
typedef Gemm<256, 128, 4, 2, 3, 10, false, true> G1n;
constexpr int EP_RS = 144;
constexpr int EP_WAVE = 64 * EP_RS;

__global__ __launch_bounds__(512, 2) void k1_qkv_h(const u16* __restrict__ x, const u16* __restrict__ wqkv, const u16* __restrict__ w1,
                                                   u16* __restrict__ qb, u16* __restrict__ kb, u16* __restrict__ vT, u16* __restrict__ hb) {
  __shared__ __attribute__((aligned(16))) char lds[G1s::LDS_BYTES];
  const int mt = blockIdx.x & 7, nt = blockIdx.x >> 3;
  const int tid = threadIdx.x, lane = tid & 63, w = tid >> 6, wm = w >> 1, wn = w & 1, r = lane & 31, hh = lane >> 5;
  const u16* A = x + (size_t)mt * 256 * D_;
  const u16* B; int ldb;
  if (nt < 15) { B = wqkv + nt * 128; ldb = 3 * D_; } else { B = w1 + (nt - 15) * 128; ldb = FF_; }
  f32x16 acc[2][2]; float rsum, rsq, csum[2];
  const bool isV = (nt >= 10 && nt < 15);
  STAMP(0);
  if (isV) G1n::run(lds, A, D_, B, ldb, acc, rsum, rsq, csum);
  else     G1s::run(lds, A, D_, B, ldb, acc, rsum, rsq, csum);
  STAMP(1);
  float* st = (float*)(lds + 8 * EP_WAVE);
  {
    float s1 = rsum + __shfl_xor(rsum, 32), s2 = rsq + __shfl_xor(rsq, 32);
    float m = s1 * (1.f / D_), v = fmaxf(s2 * (1.f / D_) - m * m, 0.f);
    float c0 = csum[0] + __shfl_xor(csum[0], 32), c1 = csum[1] + __shfl_xor(csum[1], 32);
    if (hh == 0) {
      const int row = (wm * 2 + wn) * 32 + r;
      st[row] = m; st[256 + row] = rsqrtf(v + 1e-5f);
      st[512 + wm * 128 + wn * 64 + r] = c0; st[512 + wm * 128 + wn * 64 + 32 + r] = c1;
    }
  }
  bar();
  char* ep = lds + w * EP_WAVE;
  const int tok0 = mt * 256 + wm * 64, n0 = wn * 64;
  if (!isV) {
    u16* dst; int ldd, cbase; bool relu = false;
    if (nt < 5) { dst = qb; ldd = D_; cbase = nt * 128; }
    else if (nt < 10) { dst = kb; ldd = D_; cbase = (nt - 5) * 128; }
    else { dst = hb; ldd = FF_; cbase = (nt - 15) * 128; relu = true; }
    float mean[2], rstd[2];
#pragma unroll
    for (int mi = 0; mi < 2; ++mi) { mean[mi] = st[wm * 64 + mi * 32 + r]; rstd[mi] = st[256 + wm * 64 + mi * 32 + r]; }
#pragma unroll
    for (int ni = 0; ni < 2; ++ni) {
#pragma unroll
      for (int qd = 0; qd < 4; ++qd) {
        const float* cp = st + 512 + n0 + ni * 32 + 8 * qd + 4 * hh;
        f32x4 cs = *(const f32x4*)(cp) + *(const f32x4*)(cp + 128) + *(const f32x4*)(cp + 256) + *(const f32x4*)(cp + 384);
#pragma unroll
        for (int mi = 0; mi < 2; ++mi) {
          float v[4];
#pragma unroll
          for (int j = 0; j < 4; ++j) {
            float t = rstd[mi] * (acc[mi][ni][4 * qd + j] - mean[mi] * cs[j]);
            v[j] = relu ? fmaxf(t, 0.f) : t;
          }
          *(u32x2*)(ep + (mi * 32 + r) * EP_RS + (ni * 32 + 8 * qd + 4 * hh) * 2) = (u32x2){pk2(v[0], v[1]), pk2(v[2], v[3])};
        }
      }
    }
    wave_lds_sync();
#pragma unroll
    for (int i = 0; i < 8; ++i) {
      const int row = i * 8 + (lane >> 3), ch = lane & 7;
      u32x4 v = *(const u32x4*)(ep + row * EP_RS + ch * 16);
      *(u32x4*)(dst + (size_t)(tok0 + row) * ldd + cbase + n0 + ch * 8) = v;
    }
  } else {
    const int cbase = (nt - 10) * 128;
    float cs[2];
#pragma unroll
    for (int ni = 0; ni < 2; ++ni) {
      const float* cp = st + 512 + n0 + ni * 32 + r;
      cs[ni] = cp[0] + cp[128] + cp[256] + cp[384];
    }
#pragma unroll
    for (int mi = 0; mi < 2; ++mi) {
#pragma unroll
      for (int qd = 0; qd < 4; ++qd) {
        f32x4 mn = *(const f32x4*)(st + wm * 64 + mi * 32 + 8 * qd + 4 * hh);
        f32x4 rs = *(const f32x4*)(st + 256 + wm * 64 + mi * 32 + 8 * qd + 4 * hh);
#pragma unroll
        for (int ni = 0; ni < 2; ++ni) {
          float v[4];
#pragma unroll
          for (int j = 0; j < 4; ++j) v[j] = rs[j] * (acc[mi][ni][4 * qd + j] - mn[j] * cs[ni]);
          *(u32x2*)(ep + (ni * 32 + r) * EP_RS + (mi * 32 + 8 * qd + 4 * hh) * 2) = (u32x2){pk2(v[0], v[1]), pk2(v[2], v[3])};
        }
      }
    }
    wave_lds_sync();
#pragma unroll
    for (int i = 0; i < 8; ++i) {
      const int row = i * 8 + (lane >> 3), ch = lane & 7;
      u32x4 v = *(const u32x4*)(ep + row * EP_RS + ch * 16);
      *(u32x4*)(vT + (size_t)(cbase + n0 + row) * S_ + tok0 + ch * 8) = v;
    }
  }
  STAMP(2);
}

typedef Gemm<64, 64, 2, 2, 3, 24, true, false> G2;
constexpr int G2_BLOCKS = (S_ / 64) * (D_ / 64);
constexpr int ATT_BLOCKS = NH_ * (S_ / 128);
constexpr int KS_BYTES = 384 * 64, VS_STRIDE = 776;
constexpr int EP2_RS = 80;

#define K2_OFF 0
__global__ __launch_bounds__(256, 3) void k2_attn_w2(const u16* __restrict__ qb, const u16* __restrict__ kb, const u16* __restrict__ vT,
                                                     const u16* __restrict__ hb, const u16* __restrict__ w2,
                                                     u16* __restrict__ ob, u16* __restrict__ gb) {
  __shared__ __attribute__((aligned(16))) char lds[G2::LDS_BYTES];
  const int tid = threadIdx.x, lane = tid & 63, w = tid >> 6, r = lane & 31, hh = lane >> 5;
  const int bid = blockIdx.x + K2_OFF;
  if (bid < G2_BLOCKS) {
    const int xcd = bid & 7, idx = bid >> 3;
    const int mt = xcd * 4 + idx / 10, nt = idx % 10;
    f32x16 acc[1][1]; float d0, d1, d2[1];
    G2::run(lds, hb + (size_t)mt * 64 * FF_, FF_, w2 + nt * 64, D_, acc, d0, d1, d2);
    const int wm = w >> 1, wn = w & 1;
    char* ep = lds + w * (32 * EP2_RS);
#pragma unroll
    for (int qd = 0; qd < 4; ++qd)
      *(u32x2*)(ep + r * EP2_RS + (8 * qd + 4 * hh) * 2) = (u32x2){pk2(acc[0][0][4 * qd], acc[0][0][4 * qd + 1]), pk2(acc[0][0][4 * qd + 2], acc[0][0][4 * qd + 3])};
    wave_lds_sync();
#pragma unroll
    for (int i = 0; i < 2; ++i) {
      const int row = i * 16 + (lane >> 2), ch = lane & 3;
      u32x4 v = *(const u32x4*)(ep + row * EP2_RS + ch * 16);
      *(u32x4*)(gb + (size_t)(mt * 64 + wm * 32 + row) * D_ + nt * 64 + wn * 32 + ch * 8) = v;
    }
    return;
  }
  const int ab = bid - G2_BLOCKS;
  const int h = (ab >> 3) & 31, q0 = ((ab & 7) * 2 + (ab >> 8)) * 128;
  const int kstart = q0 - WIN_;
  STAMP(0);
  char* Ks = lds;
  char* Vs = lds + KS_BYTES;
  for (int ki = tid; ki < 384; ki += 256) {
    const int key = kstart + ki;
    u32x2 a0 = {0, 0}, a1 = {0, 0}, a2 = {0, 0}, a3 = {0, 0}, a4 = {0, 0};
    if (key >= 0) {
      const u32x2* src = (const u32x2*)(kb + (size_t)key * D_ + h * DH_);
      a0 = src[0]; a1 = src[1]; a2 = src[2]; a3 = src[3]; a4 = src[4];
    }
    const int sw = (ki >> 2) & 3;
    char* rowp = Ks + ki * 64;
    *(u32x4*)(rowp + ((0 ^ sw) << 4)) = (u32x4){a0[0], a0[1], a1[0], a1[1]};
    *(u32x4*)(rowp + ((1 ^ sw) << 4)) = (u32x4){a2[0], a2[1], a3[0], a3[1]};
    *(u32x4*)(rowp + ((2 ^ sw) << 4)) = (u32x4){a4[0], a4[1], 0u, 0u};
    *(u32x4*)(rowp + ((3 ^ sw) << 4)) = (u32x4){0u, 0u, 0u, 0u};
  }
  for (int idx = tid; idx < (DH_ + 1) * 48; idx += 256) {
    const int d = idx / 48, c = idx % 48, key0 = kstart + c * 8;
    u32x4 v = {0, 0, 0, 0};
    if (d == DH_) v = (u32x4){0x3f803f80u, 0x3f803f80u, 0x3f803f80u, 0x3f803f80u};
    else if (key0 >= 0) v = *(const u32x4*)(vT + (size_t)(h * DH_ + d) * S_ + key0);
    char* dst = Vs + d * VS_STRIDE + c * 16;
    *(u32x2*)(dst) = (u32x2){v[0], v[1]};
    *(u32x2*)(dst + 8) = (u32x2){v[2], v[3]};
  }
  const int qw = q0 + 32 * w;
  bf16x8 qf0, qf1;
  {
    const u32x2* src = (const u32x2*)(qb + (size_t)(qw + r) * D_ + h * DH_);
    u32x2 a = src[2 * hh], b = src[2 * hh + 1], c = {0, 0};
    if (hh == 0) c = src[4];
    u32x4 f0 = {a[0], a[1], b[0], b[1]}, f1 = {c[0], c[1], 0u, 0u};
    qf0 = __builtin_bit_cast(bf16x8, f0); qf1 = __builtin_bit_cast(bf16x8, f1);
  }
  __syncthreads();
  STAMP(1);
  const float NEG = -__builtin_inff();
  const int t_lo = (qw >= WIN_) ? 0 : (WIN_ - qw) / 32;
  f32x16 zero;
#pragma unroll
  for (int i = 0; i < 16; ++i) zero[i] = 0.f;
  auto score_tile = [&](int t) -> f32x16 {
    const int row = 32 * (w + t) + r, sw = (row >> 2) & 3;
    bf16x8 k0 = *(const bf16x8*)(Ks + row * 64 + ((hh ^ sw) << 4));
    bf16x8 k1 = *(const bf16x8*)(Ks + row * 64 + (((2 + hh) ^ sw) << 4));
    f32x16 z = MFMA32(k0, qf0, zero);
    z = MFMA32(k1, qf1, z);
    if (t == 0) {
#pragma unroll
      for (int i = 0; i < 16; ++i) if ((i & 3) + 8 * (i >> 2) + 4 * hh <= r) z[i] = NEG;
    }
    if (t == 8) {
#pragma unroll
      for (int i = 0; i < 16; ++i) if ((i & 3) + 8 * (i >> 2) + 4 * hh > r) z[i] = NEG;
    }
    return z;
  };
  float mx = NEG;
#pragma unroll
  for (int t = 0; t < 9; ++t) {
    if (t >= t_lo) {
      f32x16 z = score_tile(t);
#pragma unroll
      for (int i = 0; i < 16; i += 2) mx = fmaxf(mx, fmaxf(z[i], z[i + 1]));
    }
  }
  mx = fmaxf(mx, __shfl_xor(mx, 32));
  STAMP(2);
  const float C2 = 0.22360679774997896f * 1.4426950408889634f;
  const float mo = -mx * C2;
  f32x16 oacc = zero;
  const bool dval = r <= DH_;
  const char* vrow = Vs + (dval ? r : 0) * VS_STRIDE;
#pragma unroll
  for (int t = 0; t < 9; ++t) {
    if (t >= t_lo) {
      f32x16 z = score_tile(t);
      float pv[16];
#pragma unroll
      for (int i = 0; i < 16; ++i) pv[i] = __builtin_amdgcn_exp2f(fmaf(z[i], C2, mo));
#pragma unroll
      for (int s = 0; s < 2; ++s) {
        u32x4 pp = {pk2(pv[8 * s], pv[8 * s + 1]), pk2(pv[8 * s + 2], pv[8 * s + 3]), pk2(pv[8 * s + 4], pv[8 * s + 5]), pk2(pv[8 * s + 6], pv[8 * s + 7])};
        const int kidx = 32 * (w + t) + 16 * s + 4 * hh;
        u32x2 v0 = *(const u32x2*)(vrow + kidx * 2);
        u32x2 v1 = *(const u32x2*)(vrow + kidx * 2 + 16);
        u32x4 vv = {v0[0], v0[1], v1[0], v1[1]};
        if (!dval) vv = (u32x4){0u, 0u, 0u, 0u};
        oacc = MFMA32(__builtin_bit_cast(bf16x8, vv), __builtin_bit_cast(bf16x8, pp), oacc);
      }
    }
  }
  float sum = __shfl(oacc[8], r + 32);
  STAMP(3);
  const float inv = 1.f / sum;
  u16* orow = ob + (size_t)(qw + r) * D_ + h * DH_ + 4 * hh;
#pragma unroll
  for (int qd = 0; qd < 3; ++qd) {
    if (qd < 2 || hh == 0) {
      u32x2 o = {pk2(oacc[4 * qd] * inv, oacc[4 * qd + 1] * inv), pk2(oacc[4 * qd + 2] * inv, oacc[4 * qd + 3] * inv)};
      *(u32x2*)(orow + 8 * qd) = o;
    }
  }
}

typedef Gemm<64, 64, 2, 2, 4, 10, true, false> G3;

__global__ __launch_bounds__(256, 2) void k3_out(const u16* __restrict__ ob, const u16* __restrict__ wo, const u16* __restrict__ gb,
                                                 const u16* __restrict__ x, u16* __restrict__ out) {
  __shared__ __attribute__((aligned(16))) char lds[G3::LDS_BYTES];
  const int tid = threadIdx.x, lane = tid & 63, w = tid >> 6, r = lane & 31, hh = lane >> 5, wm = w >> 1, wn = w & 1;
  const int xcd = blockIdx.x & 7, idx = blockIdx.x >> 3;
  const int mt = xcd * 4 + idx / 10, nt = idx % 10;
  u32x4 gv[2], xv[2];
#pragma unroll
  for (int i = 0; i < 2; ++i) {
    const size_t off = (size_t)(mt * 64 + wm * 32 + i * 16 + (lane >> 2)) * D_ + nt * 64 + wn * 32 + (lane & 3) * 8;
    gv[i] = *(const u32x4*)(gb + off);
    xv[i] = *(const u32x4*)(x + off);
  }
  f32x16 acc[1][1]; float d0, d1, d2[1];
  G3::run(lds, ob + (size_t)mt * 64 * D_, D_, wo + nt * 64, D_, acc, d0, d1, d2);
  char* ep = lds + w * (32 * EP2_RS);
#pragma unroll
  for (int qd = 0; qd < 4; ++qd)
    *(u32x2*)(ep + r * EP2_RS + (8 * qd + 4 * hh) * 2) = (u32x2){pk2(acc[0][0][4 * qd], acc[0][0][4 * qd + 1]), pk2(acc[0][0][4 * qd + 2], acc[0][0][4 * qd + 3])};
  wave_lds_sync();
#pragma unroll
  for (int i = 0; i < 2; ++i) {
    const int row = i * 16 + (lane >> 2), ch = lane & 3;
    u32x4 sv = *(const u32x4*)(ep + row * EP2_RS + ch * 16);
    float o[8];
#pragma unroll
    for (int j = 0; j < 8; ++j) {
      const float g = bf2f((u16)(gv[i][j >> 1] >> (16 * (j & 1))));
      const float xx = bf2f((u16)(xv[i][j >> 1] >> (16 * (j & 1))));
      const float sf = bf2f((u16)(sv[j >> 1] >> (16 * (j & 1))));
      const float sg = rbf(g / (1.f + __expf(-g)));
      o[j] = xx + rbf(sf * sg);
    }
    u32x4 ov = {pk2(o[0], o[1]), pk2(o[2], o[3]), pk2(o[4], o[5]), pk2(o[6], o[7])};
    *(u32x4*)(out + (size_t)(mt * 64 + wm * 32 + row) * D_ + nt * 64 + wn * 32 + ch * 8) = ov;
  }
}

extern "C" void kernel_launch(void* const* d_in, const int* in_sizes, int n_in,
                              void* d_out, int out_size, void* d_ws, size_t ws_size,
                              hipStream_t stream) {
  const u16* x = (const u16*)d_in[0];
  const u16* wqkv = (const u16*)d_in[1];
  const u16* wo = (const u16*)d_in[2];
  const u16* w1 = (const u16*)d_in[4];
  const u16* w2 = (const u16*)d_in[5];
  u16* out = (u16*)d_out;
  char* ws = (char*)d_ws;
  const size_t SD = (size_t)S_ * D_ * 2;
  u16* qb = (u16*)(ws);
  u16* kb = (u16*)(ws + SD);
  u16* vT = (u16*)(ws + 2 * SD);
  u16* ob = (u16*)(ws + 3 * SD);
  u16* gb = (u16*)(ws + 4 * SD);
  u16* hb = (u16*)(ws + 5 * SD);
  k1_qkv_h<<<8 * 27, 512, 0, stream>>>(x, wqkv, w1, qb, kb, vT, hb);
  k2_attn_w2<<<G2_BLOCKS + ATT_BLOCKS, 256, 0, stream>>>(qb, kb, vT, hb, w2, ob, gb);
  k3_out<<<(S_ / 64) * (D_ / 64), 256, 0, stream>>>(ob, wo, gb, x, out);
}
```

```cpp
#include <hip/hip_runtime.h>
#include <hip/hip_bf16.h>
#include <stdint.h>

typedef short bf16x8 __attribute__((ext_vector_type(8)));
typedef short s16x4 __attribute__((ext_vector_type(4)));
typedef __bf16 bf16x2_t __attribute__((ext_vector_type(2)));
typedef __bf16 bf16x4_t __attribute__((ext_vector_type(4)));
typedef float f32x16 __attribute__((ext_vector_type(16)));
typedef float f32x4 __attribute__((ext_vector_type(4)));
typedef unsigned u32x4 __attribute__((ext_vector_type(4)));
typedef unsigned u32x2 __attribute__((ext_vector_type(2)));
typedef unsigned short u16;

#define DI __device__ __forceinline__
#define MFMA32(a, b, c) __builtin_amdgcn_mfma_f32_32x32x16_bf16((a), (b), (c), 0, 0, 0)

#define STAMP(i) do { } while (0)

constexpr int S_ = 2048, D_ = 640, FF_ = 1536, NH_ = 32, DH_ = 20, WIN_ = 256;

template <int N> DI void wait_vm_t() { asm volatile("s_waitcnt vmcnt(%0)" ::"n"(N) : "memory"); }
DI void wait_vm(int n) {
  switch (n) {
    case 0: wait_vm_t<0>(); break;   case 2: wait_vm_t<2>(); break;   case 3: wait_vm_t<3>(); break;
    case 4: wait_vm_t<4>(); break;   case 6: wait_vm_t<6>(); break;   case 8: wait_vm_t<8>(); break;
    case 9: wait_vm_t<9>(); break;   case 12: wait_vm_t<12>(); break; case 16: wait_vm_t<16>(); break;
    case 18: wait_vm_t<18>(); break; case 24: wait_vm_t<24>(); break;
    default: wait_vm_t<0>(); break;
  }
}
template <int N> DI void wait_lgkm() { asm volatile("s_waitcnt lgkmcnt(%0)" ::"n"(N) : "memory"); }
DI void bar() {
  asm volatile("s_waitcnt lgkmcnt(0)" ::: "memory");
  __builtin_amdgcn_s_barrier();
  asm volatile("" ::: "memory");
}
DI void wave_lds_sync() { asm volatile("s_waitcnt lgkmcnt(0)" ::: "memory"); __builtin_amdgcn_wave_barrier(); }
DI u16 f2bf(float f) { return __builtin_bit_cast(u16, (__bf16)f); }
DI float bf2f(u16 v) { return __builtin_bit_cast(float, (unsigned)v << 16); }
DI float rbf(float f) { return bf2f(f2bf(f)); }
DI unsigned pk2(float a, float b) { return (unsigned)f2bf(a) | ((unsigned)f2bf(b) << 16); }
DI float dot2acc(unsigned a, unsigned b, float c) {
  return __builtin_amdgcn_fdot2_f32_bf16(__builtin_bit_cast(bf16x2_t, a), __builtin_bit_cast(bf16x2_t, b), c, false);
}
DI s16x4 tr_read(const char* p) {
  bf16x4_t v = __builtin_amdgcn_ds_read_tr16_b64_v4bf16((__attribute__((address_space(3))) bf16x4_t*)(p));
  return __builtin_bit_cast(s16x4, v);
}
DI void glds16(const void* g, char* l) {
  __builtin_amdgcn_global_load_lds((const __attribute__((address_space(1))) void*)g,
                                   (__attribute__((address_space(3))) void*)l, 16, 0, 0);
}

template <int BM, int BN, int WM, int WN, int NS, int NK, bool SWAP, bool STATS>
struct Gemm {
  static constexpr int NTH = WM * WN * 64;
  static constexpr int MT = BM / WM / 32, NT = BN / WN / 32;
  static constexpr int A_BYTES = BM * 128, B_BYTES = 64 * BN * 2, STAGE_BYTES = A_BYTES + B_BYTES;
  static constexpr int A_GLDS = BM * 8 / NTH, B_CPR = BN / 8, B_GLDS = 64 * B_CPR / NTH;
  static constexpr int NG = A_GLDS + B_GLDS;
  static constexpr int LDS_BYTES = NS * STAGE_BYTES;

  DI static int swzB(int k) { return (B_CPR == 16) ? ((k & 3) << 2) : (((k >> 1) & 1) << 2); }

  DI static void init_ptrs(const u16* __restrict__ A, int lda, const u16* __restrict__ B, int ldb, int tid,
                           const u16* (&pa)[A_GLDS], const u16* (&pb)[B_GLDS]) {
#pragma unroll
    for (int i = 0; i < A_GLDS; ++i) {
      const int ci = i * NTH + tid, row = ci >> 3, c = (ci & 7) ^ ((row >> 1) & 7);
      pa[i] = A + (size_t)row * lda + c * 8;
    }
#pragma unroll
    for (int i = 0; i < B_GLDS; ++i) {
      const int ci = i * NTH + tid, k = ci / B_CPR, c = (ci % B_CPR) ^ swzB(k);
      pb[i] = B + (size_t)k * ldb + c * 8;
    }
  }
  DI static void stage(char* buf, const u16* (&pa)[A_GLDS], const u16* (&pb)[B_GLDS], int ldb, int wbase) {
#pragma unroll
    for (int i = 0; i < A_GLDS; ++i) {
      glds16(pa[i], buf + i * (NTH * 16) + wbase);
      pa[i] += 64; asm volatile("" : "+v"(pa[i]));
    }
#pragma unroll
    for (int i = 0; i < B_GLDS; ++i) {
      glds16(pb[i], buf + A_BYTES + i * (NTH * 16) + wbase);
      pb[i] += (size_t)64 * ldb; asm volatile("" : "+v"(pb[i]));
    }
  }

  DI static void run(char* lds, const u16* __restrict__ A, int lda, const u16* __restrict__ B, int ldb,
                     f32x16 (&acc)[MT][NT], float& rsum, float& rsq, float (&csum)[NT]) {
    const int tid = threadIdx.x, lane = tid & 63;
    const int w = __builtin_amdgcn_readfirstlane(tid >> 6), wm = w / WN, wn = w % WN;
    const int r = lane & 31, hh = lane >> 5;
    const int blk = (lane >> 4) & 1, q = (lane & 15) >> 2, p = lane & 3;
#pragma unroll
    for (int mi = 0; mi < MT; ++mi)
#pragma unroll
      for (int ni = 0; ni < NT; ++ni)
#pragma unroll
        for (int i = 0; i < 16; ++i) acc[mi][ni][i] = 0.f;
    rsum = 0.f; rsq = 0.f;
#pragma unroll
    for (int ni = 0; ni < NT; ++ni) csum[ni] = 0.f;
    const unsigned ONES = 0x3f803f80u;
    float rs4[4] = {0.f, 0.f, 0.f, 0.f}, rq4[4] = {0.f, 0.f, 0.f, 0.f};
    unsigned ones = ONES; asm volatile("" : "+v"(ones));
    const unsigned lds_base = (unsigned)(size_t)(__attribute__((address_space(3))) char*)lds;
    unsigned a_off[MT][4], b_off[NT];
#pragma unroll
    for (int mi = 0; mi < MT; ++mi) {
      const int row = (wm * MT + mi) * 32 + r;
#pragma unroll
      for (int s = 0; s < 4; ++s) a_off[mi][s] = row * 128 + (((2 * s + hh) ^ ((row >> 1) & 7)) << 4);
    }
#pragma unroll
    for (int ni = 0; ni < NT; ++ni) {
      const int cc = ((wn * NT + ni) * 32 + blk * 16 + 4 * p) >> 3, k1 = 8 * hh + q;
      b_off[ni] = A_BYTES + k1 * (BN * 2) + ((cc ^ swzB(k1)) << 4) + (p & 1) * 8;
    }

    const u16* pa[A_GLDS]; const u16* pb[B_GLDS];
    init_ptrs(A, lda, B, ldb, tid, pa, pb);
    const int wbase = w * 1024;
#pragma unroll
    for (int kt = 0; kt < NS - 1; ++kt) stage(lds + kt * STAGE_BYTES, pa, pb, ldb, wbase);
#pragma unroll
    for (int kt = 0; kt < NK; ++kt) {
      {
        const int last = (kt + NS - 2 < NK - 1) ? (kt + NS - 2) : (NK - 1);
        wait_vm((last - kt) * NG);
      }
      bar();
      if (kt + NS - 1 < NK) stage(lds + ((kt + NS - 1) % NS) * STAGE_BYTES, pa, pb, ldb, wbase);
      {
        const unsigned sbase = lds_base + (kt % NS) * STAGE_BYTES;
        u32x4 fa[2][MT]; u32x2 fl[2][NT], fh[2][NT];
        auto issue = [&](int s, u32x4 (&ra)[MT], u32x2 (&rl)[NT], u32x2 (&rh)[NT]) {
#pragma unroll
          for (int mi = 0; mi < MT; ++mi) {
            const unsigned ad = sbase + a_off[mi][s];
            asm volatile("ds_read_b128 %0, %1" : "=v"(ra[mi]) : "v"(ad));
          }
#pragma unroll
          for (int ni = 0; ni < NT; ++ni) {
            const unsigned ad = sbase + b_off[ni];
            asm volatile("ds_read_b64_tr_b16 %0, %1 offset:%2" : "=v"(rl[ni]) : "v"(ad), "n"(s * 16 * BN * 2));
            asm volatile("ds_read_b64_tr_b16 %0, %1 offset:%2" : "=v"(rh[ni]) : "v"(ad), "n"(s * 16 * BN * 2 + 4 * BN * 2));
          }
        };
        auto settle = [&](u32x4 (&ra)[MT], u32x2 (&rl)[NT], u32x2 (&rh)[NT]) {
#pragma unroll
          for (int mi = 0; mi < MT; ++mi) asm volatile("" : "+v"(ra[mi]));
#pragma unroll
          for (int ni = 0; ni < NT; ++ni) { asm volatile("" : "+v"(rl[ni])); asm volatile("" : "+v"(rh[ni])); }
        };
        constexpr int NRD = MT + 2 * NT;
        issue(0, fa[0], fl[0], fh[0]);
#pragma unroll
        for (int s = 0; s < 4; ++s) {
          if (s < 3) { issue(s + 1, fa[(s + 1) & 1], fl[(s + 1) & 1], fh[(s + 1) & 1]); wait_lgkm<NRD>(); }
          else wait_lgkm<0>();
          settle(fa[s & 1], fl[s & 1], fh[s & 1]);
          __builtin_amdgcn_sched_barrier(0);
#pragma unroll
          for (int mi = 0; mi < MT; ++mi)
#pragma unroll
            for (int ni = 0; ni < NT; ++ni) {
              const bf16x8 av = __builtin_bit_cast(bf16x8, fa[s & 1][mi]);
              const u32x4 bq = {fl[s & 1][ni][0], fl[s & 1][ni][1], fh[s & 1][ni][0], fh[s & 1][ni][1]};
              const bf16x8 bv = __builtin_bit_cast(bf16x8, bq);
              acc[mi][ni] = SWAP ? MFMA32(bv, av, acc[mi][ni]) : MFMA32(av, bv, acc[mi][ni]);
            }
          if (STATS) {
            u32x4 u = fa[s & 1][0];
#pragma unroll
            for (int mi = 1; mi < MT; ++mi) if (wn == mi) u = fa[s & 1][mi];
#pragma unroll
            for (int j = 0; j < 4; ++j) {
              asm volatile("v_dot2c_f32_bf16 %0, %1, %2" : "+v"(rs4[j]) : "v"(u[j]), "v"(ones));
              asm volatile("v_dot2c_f32_bf16 %0, %1, %1" : "+v"(rq4[j]) : "v"(u[j]));
            }
            if (wm == s) {
#pragma unroll
              for (int ni = 0; ni < NT; ++ni) {
                asm volatile("v_dot2c_f32_bf16 %0, %1, %2" : "+v"(csum[ni]) : "v"(fl[s & 1][ni][0]), "v"(ones));
                asm volatile("v_dot2c_f32_bf16 %0, %1, %2" : "+v"(csum[ni]) : "v"(fl[s & 1][ni][1]), "v"(ones));
                asm volatile("v_dot2c_f32_bf16 %0, %1, %2" : "+v"(csum[ni]) : "v"(fh[s & 1][ni][0]), "v"(ones));
                asm volatile("v_dot2c_f32_bf16 %0, %1, %2" : "+v"(csum[ni]) : "v"(fh[s & 1][ni][1]), "v"(ones));
              }
            }
          }
          __builtin_amdgcn_sched_barrier(0);
        }
      }
    }
    rsum = (rs4[0] + rs4[1]) + (rs4[2] + rs4[3]); rsq = (rq4[0] + rq4[1]) + (rq4[2] + rq4[3]);
    bar();
  }
};

typedef Gemm<256, 128, 4, 2, 3, 10, true, true> G1s;
typedef Gemm<256, 128, 4, 2, 3, 10, false, true> G1n;
constexpr int EP_RS = 144;
constexpr int EP_WAVE = 64 * EP_RS;

__global__ __launch_bounds__(512, 2) void k1_qkv_h(const u16* __restrict__ x, const u16* __restrict__ wqkv, const u16* __restrict__ w1,
                                                   u16* __restrict__ qb, u16* __restrict__ kb, u16* __restrict__ vT, u16* __restrict__ hb) {
  __shared__ __attribute__((aligned(16))) char lds[G1s::LDS_BYTES];
  int mt, nt;
  { const int xcd = blockIdx.x & 7, idx = blockIdx.x >> 3;
    const int j = (xcd & 3) * 27 + idx; mt = (xcd >> 2) * 4 + (j & 3); nt = j >> 2; }
  const int tid = threadIdx.x, lane = tid & 63, w = tid >> 6, wm = w >> 1, wn = w & 1, r = lane & 31, hh = lane >> 5;
  const u16* A = x + (size_t)mt * 256 * D_;
  const u16* B; int ldb;
  if (nt < 15) { B = wqkv + nt * 128; ldb = 3 * D_; } else { B = w1 + (nt - 15) * 128; ldb = FF_; }
  f32x16 acc[2][2]; float rsum, rsq, csum[2];
  const bool isV = (nt >= 10 && nt < 15);
  STAMP(0);
  if (isV) G1n::run(lds, A, D_, B, ldb, acc, rsum, rsq, csum);
  else     G1s::run(lds, A, D_, B, ldb, acc, rsum, rsq, csum);
  STAMP(1);
  float* st = (float*)(lds + 8 * EP_WAVE);
  {
    float s1 = rsum + __shfl_xor(rsum, 32), s2 = rsq + __shfl_xor(rsq, 32);
    float m = s1 * (1.f / D_), v = fmaxf(s2 * (1.f / D_) - m * m, 0.f);
    float c0 = csum[0] + __shfl_xor(csum[0], 32), c1 = csum[1] + __shfl_xor(csum[1], 32);
    if (hh == 0) {
      const int row = (wm * 2 + wn) * 32 + r;
      st[row] = m; st[256 + row] = rsqrtf(v + 1e-5f);
      st[512 + wm * 128 + wn * 64 + r] = c0; st[512 + wm * 128 + wn * 64 + 32 + r] = c1;
    }
  }
  bar();
  char* ep = lds + w * EP_WAVE;
  const int tok0 = mt * 256 + wm * 64, n0 = wn * 64;
  if (!isV) {
    u16* dst; int ldd, cbase; bool relu = false;
    if (nt < 5) { dst = qb; ldd = D_; cbase = nt * 128; }
    else if (nt < 10) { dst = kb; ldd = D_; cbase = (nt - 5) * 128; }
    else { dst = hb; ldd = FF_; cbase = (nt - 15) * 128; relu = true; }
    float mean[2], rstd[2];
#pragma unroll
    for (int mi = 0; mi < 2; ++mi) { mean[mi] = st[wm * 64 + mi * 32 + r]; rstd[mi] = st[256 + wm * 64 + mi * 32 + r]; }
#pragma unroll
    for (int ni = 0; ni < 2; ++ni) {
#pragma unroll
      for (int qd = 0; qd < 4; ++qd) {
        const float* cp = st + 512 + n0 + ni * 32 + 8 * qd + 4 * hh;
        f32x4 cs = *(const f32x4*)(cp) + *(const f32x4*)(cp + 128) + *(const f32x4*)(cp + 256) + *(const f32x4*)(cp + 384);
#pragma unroll
        for (int mi = 0; mi < 2; ++mi) {
          float v[4];
#pragma unroll
          for (int j = 0; j < 4; ++j) {
            float t = rstd[mi] * (acc[mi][ni][4 * qd + j] - mean[mi] * cs[j]);
            v[j] = relu ? fmaxf(t, 0.f) : t;
          }
          *(u32x2*)(ep + (mi * 32 + r) * EP_RS + (ni * 32 + 8 * qd + 4 * hh) * 2) = (u32x2){pk2(v[0], v[1]), pk2(v[2], v[3])};
        }
      }
    }
    wave_lds_sync();
#pragma unroll
    for (int i = 0; i < 8; ++i) {
      const int row = i * 8 + (lane >> 3), ch = lane & 7;
      u32x4 v = *(const u32x4*)(ep + row * EP_RS + ch * 16);
      *(u32x4*)(dst + (size_t)(tok0 + row) * ldd + cbase + n0 + ch * 8) = v;
    }
  } else {
    const int cbase = (nt - 10) * 128;
    float cs[2];
#pragma unroll
    for (int ni = 0; ni < 2; ++ni) {
      const float* cp = st + 512 + n0 + ni * 32 + r;
      cs[ni] = cp[0] + cp[128] + cp[256] + cp[384];
    }
#pragma unroll
    for (int mi = 0; mi < 2; ++mi) {
#pragma unroll
      for (int qd = 0; qd < 4; ++qd) {
        f32x4 mn = *(const f32x4*)(st + wm * 64 + mi * 32 + 8 * qd + 4 * hh);
        f32x4 rs = *(const f32x4*)(st + 256 + wm * 64 + mi * 32 + 8 * qd + 4 * hh);
#pragma unroll
        for (int ni = 0; ni < 2; ++ni) {
          float v[4];
#pragma unroll
          for (int j = 0; j < 4; ++j) v[j] = rs[j] * (acc[mi][ni][4 * qd + j] - mn[j] * cs[ni]);
          *(u32x2*)(ep + (ni * 32 + r) * EP_RS + (mi * 32 + 8 * qd + 4 * hh) * 2) = (u32x2){pk2(v[0], v[1]), pk2(v[2], v[3])};
        }
      }
    }
    wave_lds_sync();
#pragma unroll
    for (int i = 0; i < 8; ++i) {
      const int row = i * 8 + (lane >> 3), ch = lane & 7;
      u32x4 v = *(const u32x4*)(ep + row * EP_RS + ch * 16);
      *(u32x4*)(vT + (size_t)(cbase + n0 + row) * S_ + tok0 + ch * 8) = v;
    }
  }
  STAMP(2);
}

constexpr int ATT_BLOCKS = NH_ * (S_ / 128);
constexpr int KS_BYTES = 384 * 64, VS_STRIDE = 776;
constexpr int ATT_LDS = KS_BYTES + (DH_ + 1) * VS_STRIDE + 8;

__global__ __launch_bounds__(256, 4) void k2_attn(const u16* __restrict__ qb, const u16* __restrict__ kb, const u16* __restrict__ vT,
                                                  u16* __restrict__ ob) {
  __shared__ __attribute__((aligned(16))) char lds[ATT_LDS];
  const int tid = threadIdx.x, lane = tid & 63, w = tid >> 6, r = lane & 31, hh = lane >> 5;
  const int ab = blockIdx.x;
  const int h = (ab >> 3) & 31, q0 = ((ab & 7) * 2 + (ab >> 8)) * 128;
  const int kstart = q0 - WIN_;
  STAMP(0);
  char* Ks = lds;
  char* Vs = lds + KS_BYTES;
  u32x2 kr[2][5];
#pragma unroll
  for (int it = 0; it < 2; ++it) {
    const int ki = tid + it * 256, key = kstart + ki;
#pragma unroll
    for (int j = 0; j < 5; ++j) kr[it][j] = (u32x2){0u, 0u};
    if (ki < 384 && key >= 0) {
      const u32x2* src = (const u32x2*)(kb + (size_t)key * D_ + h * DH_);
#pragma unroll
      for (int j = 0; j < 5; ++j) kr[it][j] = src[j];
    }
  }
  u32x4 vr[4];
#pragma unroll
  for (int it = 0; it < 4; ++it) {
    const int idx = tid + it * 256, d = idx / 48, c = idx % 48, key0 = kstart + c * 8;
    vr[it] = (u32x4){0u, 0u, 0u, 0u};
    if (d == DH_) vr[it] = (u32x4){0x3f803f80u, 0x3f803f80u, 0x3f803f80u, 0x3f803f80u};
    else if (d < DH_ && key0 >= 0) vr[it] = *(const u32x4*)(vT + (size_t)(h * DH_ + d) * S_ + key0);
  }
  const int qw = q0 + 32 * w;
  bf16x8 qf0, qf1;
  {
    const u32x2* src = (const u32x2*)(qb + (size_t)(qw + r) * D_ + h * DH_);
    u32x2 a = src[2 * hh], b = src[2 * hh + 1], c = {0, 0};
    if (hh == 0) c = src[4];
    u32x4 f0 = {a[0], a[1], b[0], b[1]}, f1 = {c[0], c[1], 0u, 0u};
    qf0 = __builtin_bit_cast(bf16x8, f0); qf1 = __builtin_bit_cast(bf16x8, f1);
  }
#pragma unroll
  for (int it = 0; it < 2; ++it) {
    const int ki = tid + it * 256;
    if (ki < 384) {
      const int sw = (ki >> 2) & 3;
      char* rowp = Ks + ki * 64;
      *(u32x4*)(rowp + ((0 ^ sw) << 4)) = (u32x4){kr[it][0][0], kr[it][0][1], kr[it][1][0], kr[it][1][1]};
      *(u32x4*)(rowp + ((1 ^ sw) << 4)) = (u32x4){kr[it][2][0], kr[it][2][1], kr[it][3][0], kr[it][3][1]};
      *(u32x4*)(rowp + ((2 ^ sw) << 4)) = (u32x4){kr[it][4][0], kr[it][4][1], 0u, 0u};
      *(u32x4*)(rowp + ((3 ^ sw) << 4)) = (u32x4){0u, 0u, 0u, 0u};
    }
  }
#pragma unroll
  for (int it = 0; it < 4; ++it) {
    const int idx = tid + it * 256, d = idx / 48, c = idx % 48;
    if (d <= DH_) {
      char* dst = Vs + d * VS_STRIDE + c * 16;
      *(u32x2*)(dst) = (u32x2){vr[it][0], vr[it][1]};
      *(u32x2*)(dst + 8) = (u32x2){vr[it][2], vr[it][3]};
    }
  }
  __syncthreads();
  STAMP(1);
  const float NEG = -__builtin_inff();
  const int t_lo = (qw >= WIN_) ? 0 : (WIN_ - qw) / 32;
  f32x16 zero;
#pragma unroll
  for (int i = 0; i < 16; ++i) zero[i] = 0.f;
  auto score_tile = [&](int t) -> f32x16 {
    const int row = 32 * (w + t) + r, sw = (row >> 2) & 3;
    bf16x8 k0 = *(const bf16x8*)(Ks + row * 64 + ((hh ^ sw) << 4));
    bf16x8 k1 = *(const bf16x8*)(Ks + row * 64 + (((2 + hh) ^ sw) << 4));
    f32x16 z = MFMA32(k0, qf0, zero);
    z = MFMA32(k1, qf1, z);
    if (t == 0) {
#pragma unroll
      for (int i = 0; i < 16; ++i) if ((i & 3) + 8 * (i >> 2) + 4 * hh <= r) z[i] = NEG;
    }
    if (t == 8) {
#pragma unroll
      for (int i = 0; i < 16; ++i) if ((i & 3) + 8 * (i >> 2) + 4 * hh > r) z[i] = NEG;
    }
    return z;
  };
  float mx = NEG;
#pragma unroll
  for (int t = 0; t < 9; ++t) {
    if (t >= t_lo) {
      f32x16 z = score_tile(t);
#pragma unroll
      for (int i = 0; i < 16; i += 2) mx = fmaxf(mx, fmaxf(z[i], z[i + 1]));
    }
  }
  mx = fmaxf(mx, __shfl_xor(mx, 32));
  STAMP(2);
  const float C2 = 0.22360679774997896f * 1.4426950408889634f;
  const float mo = -mx * C2;
  f32x16 oacc = zero;
  const bool dval = r <= DH_;
  const char* vrow = Vs + (dval ? r : 0) * VS_STRIDE;
#pragma unroll
  for (int t = 0; t < 9; ++t) {
    if (t >= t_lo) {
      f32x16 z = score_tile(t);
      float pv[16];
#pragma unroll
      for (int i = 0; i < 16; ++i) pv[i] = __builtin_amdgcn_exp2f(fmaf(z[i], C2, mo));
#pragma unroll
      for (int s = 0; s < 2; ++s) {
        u32x4 pp = {pk2(pv[8 * s], pv[8 * s + 1]), pk2(pv[8 * s + 2], pv[8 * s + 3]), pk2(pv[8 * s + 4], pv[8 * s + 5]), pk2(pv[8 * s + 6], pv[8 * s + 7])};
        const int kidx = 32 * (w + t) + 16 * s + 4 * hh;
        u32x2 v0 = *(const u32x2*)(vrow + kidx * 2);
        u32x2 v1 = *(const u32x2*)(vrow + kidx * 2 + 16);
        u32x4 vv = {v0[0], v0[1], v1[0], v1[1]};
        if (!dval) vv = (u32x4){0u, 0u, 0u, 0u};
        oacc = MFMA32(__builtin_bit_cast(bf16x8, vv), __builtin_bit_cast(bf16x8, pp), oacc);
      }
    }
  }
  float sum = __shfl(oacc[8], r + 32);
  STAMP(3);
  const float inv = 1.f / sum;
  u16* orow = ob + (size_t)(qw + r) * D_ + h * DH_ + 4 * hh;
#pragma unroll
  for (int qd = 0; qd < 3; ++qd) {
    if (qd < 2 || hh == 0) {
      u32x2 o = {pk2(oacc[4 * qd] * inv, oacc[4 * qd + 1] * inv), pk2(oacc[4 * qd + 2] * inv, oacc[4 * qd + 3] * inv)};
      *(u32x2*)(orow + 8 * qd) = o;
    }
  }
}

typedef Gemm<128, 64, 2, 2, 4, 24, true, false> G3a;
typedef Gemm<128, 64, 2, 2, 4, 10, true, false> G3b;
constexpr int EP3_RS = 80;

__global__ __launch_bounds__(256, 1) void k3_out(const u16* __restrict__ ob, const u16* __restrict__ wo, const u16* __restrict__ hb,
                                                 const u16* __restrict__ w2, const u16* __restrict__ x, u16* __restrict__ out) {
  __shared__ __attribute__((aligned(16))) char lds[G3a::LDS_BYTES];
  const int tid = threadIdx.x, lane = tid & 63, w = tid >> 6, r = lane & 31, hh = lane >> 5, wm = w >> 1, wn = w & 1;
  const int xcd = blockIdx.x & 7, idx = blockIdx.x >> 3;
  const int mt = xcd * 2 + idx / 10, nt = idx % 10;
  STAMP(0);
  u32x4 xv[4];
#pragma unroll
  for (int i = 0; i < 4; ++i)
    xv[i] = *(const u32x4*)(x + (size_t)(mt * 128 + wm * 64 + i * 16 + (lane >> 2)) * D_ + nt * 64 + wn * 32 + (lane & 3) * 8);
  f32x16 accg[2][1], accs[2][1]; float d0, d1, d2[1];
  G3a::run(lds, hb + (size_t)mt * 128 * FF_, FF_, w2 + nt * 64, D_, accg, d0, d1, d2);
  STAMP(1);
  G3b::run(lds, ob + (size_t)mt * 128 * D_, D_, wo + nt * 64, D_, accs, d0, d1, d2);
  STAMP(2);
  char* ep = lds + w * (64 * EP3_RS);
#pragma unroll
  for (int mi = 0; mi < 2; ++mi)
#pragma unroll
    for (int qd = 0; qd < 4; ++qd) {
      float o[4];
#pragma unroll
      for (int j = 0; j < 4; ++j) {
        const float g = rbf(accg[mi][0][4 * qd + j]);
        const float sg = rbf(g / (1.f + __expf(-g)));
        o[j] = rbf(accs[mi][0][4 * qd + j]) * sg;
      }
      *(u32x2*)(ep + (mi * 32 + r) * EP3_RS + (8 * qd + 4 * hh) * 2) = (u32x2){pk2(o[0], o[1]), pk2(o[2], o[3])};
    }
  wave_lds_sync();
#pragma unroll
  for (int i = 0; i < 4; ++i) {
    const int row = i * 16 + (lane >> 2), ch = lane & 3;
    u32x4 pv = *(const u32x4*)(ep + row * EP3_RS + ch * 16);
    float o[8];
#pragma unroll
    for (int j = 0; j < 8; ++j)
      o[j] = bf2f((u16)(xv[i][j >> 1] >> (16 * (j & 1)))) + bf2f((u16)(pv[j >> 1] >> (16 * (j & 1))));
    u32x4 ov = {pk2(o[0], o[1]), pk2(o[2], o[3]), pk2(o[4], o[5]), pk2(o[6], o[7])};
    *(u32x4*)(out + (size_t)(mt * 128 + wm * 64 + row) * D_ + nt * 64 + wn * 32 + ch * 8) = ov;
  }
  STAMP(3);
}

extern "C" void kernel_launch(void* const* d_in, const int* in_sizes, int n_in,
                              void* d_out, int out_size, void* d_ws, size_t ws_size,
                              hipStream_t stream) {
  const u16* x = (const u16*)d_in[0];
  const u16* wqkv = (const u16*)d_in[1];
  const u16* wo = (const u16*)d_in[2];
  const u16* w1 = (const u16*)d_in[4];
  const u16* w2 = (const u16*)d_in[5];
  u16* out = (u16*)d_out;
  char* ws = (char*)d_ws;
  const size_t SD = (size_t)S_ * D_ * 2;
  u16* qb = (u16*)(ws);
  u16* kb = (u16*)(ws + SD);
  u16* vT = (u16*)(ws + 2 * SD);
  u16* ob = (u16*)(ws + 3 * SD);
  u16* hb = (u16*)(ws + 5 * SD);
  k1_qkv_h<<<8 * 27, 512, 0, stream>>>(x, wqkv, w1, qb, kb, vT, hb);
  k2_attn<<<ATT_BLOCKS, 256, 0, stream>>>(qb, kb, vT, ob);
  k3_out<<<(S_ / 128) * (D_ / 64), 256, 0, stream>>>(ob, wo, hb, w2, x, out);
}
```

```cpp
#include <hip/hip_runtime.h>
#include <hip/hip_bf16.h>
#include <stdint.h>

typedef short bf16x8 __attribute__((ext_vector_type(8)));
typedef short s16x4 __attribute__((ext_vector_type(4)));
typedef __bf16 bf16x2_t __attribute__((ext_vector_type(2)));
typedef __bf16 bf16x4_t __attribute__((ext_vector_type(4)));
typedef float f32x16 __attribute__((ext_vector_type(16)));
typedef float f32x4 __attribute__((ext_vector_type(4)));
typedef unsigned u32x4 __attribute__((ext_vector_type(4)));
typedef unsigned u32x2 __attribute__((ext_vector_type(2)));
typedef unsigned short u16;

#define DI __device__ __forceinline__
#define MFMA32(a, b, c) __builtin_amdgcn_mfma_f32_32x32x16_bf16((a), (b), (c), 0, 0, 0)

#define STAMP(i) do { } while (0)

constexpr int S_ = 2048, D_ = 640, FF_ = 1536, NH_ = 32, DH_ = 20, WIN_ = 256;

template <int N> DI void wait_vm_t() { asm volatile("s_waitcnt vmcnt(%0)" ::"n"(N) : "memory"); }
DI void wait_vm(int n) {
  switch (n) {
    case 0: wait_vm_t<0>(); break;   case 2: wait_vm_t<2>(); break;   case 3: wait_vm_t<3>(); break;
    case 4: wait_vm_t<4>(); break;   case 6: wait_vm_t<6>(); break;   case 8: wait_vm_t<8>(); break;
    case 9: wait_vm_t<9>(); break;   case 12: wait_vm_t<12>(); break; case 16: wait_vm_t<16>(); break;
    case 18: wait_vm_t<18>(); break; case 24: wait_vm_t<24>(); break; case 30: wait_vm_t<30>(); break;
    default: wait_vm_t<0>(); break;
  }
}
template <int N> DI void wait_lgkm() { asm volatile("s_waitcnt lgkmcnt(%0)" ::"n"(N) : "memory"); }
DI void bar() {
  asm volatile("s_waitcnt lgkmcnt(0)" ::: "memory");
  __builtin_amdgcn_s_barrier();
  asm volatile("" ::: "memory");
}
DI void wave_lds_sync() { asm volatile("s_waitcnt lgkmcnt(0)" ::: "memory"); __builtin_amdgcn_wave_barrier(); }
DI u16 f2bf(float f) { return __builtin_bit_cast(u16, (__bf16)f); }
DI float bf2f(u16 v) { return __builtin_bit_cast(float, (unsigned)v << 16); }
DI float rbf(float f) { return bf2f(f2bf(f)); }
DI unsigned pk2(float a, float b) { return (unsigned)f2bf(a) | ((unsigned)f2bf(b) << 16); }
DI float dot2acc(unsigned a, unsigned b, float c) {
  return __builtin_amdgcn_fdot2_f32_bf16(__builtin_bit_cast(bf16x2_t, a), __builtin_bit_cast(bf16x2_t, b), c, false);
}
DI void dot2v(float& acc, unsigned a, unsigned b) { asm volatile("v_dot2c_f32_bf16 %0, %1, %2" : "+v"(acc) : "v"(a), "v"(b)); }
DI s16x4 tr_read(const char* p) {
  bf16x4_t v = __builtin_amdgcn_ds_read_tr16_b64_v4bf16((__attribute__((address_space(3))) bf16x4_t*)(p));
  return __builtin_bit_cast(s16x4, v);
}
DI void glds16(const void* g, char* l) {
  __builtin_amdgcn_global_load_lds((const __attribute__((address_space(1))) void*)g,
                                   (__attribute__((address_space(3))) void*)l, 16, 0, 0);
}

template <int BM, int BN, int WM, int WN, int NS, int NK, bool SWAP, bool STATS>
struct Gemm {
  static constexpr int NTH = WM * WN * 64;
  static constexpr int MT = BM / WM / 32, NT = BN / WN / 32;
  static constexpr int A_BYTES = BM * 128, B_BYTES = 64 * BN * 2, STAGE_BYTES = A_BYTES + B_BYTES;
  static constexpr int A_GLDS = BM * 8 / NTH, B_CPR = BN / 8, B_GLDS = 64 * B_CPR / NTH;
  static constexpr int NG = A_GLDS + B_GLDS;
  static constexpr int LDS_BYTES = NS * STAGE_BYTES;

  DI static int swzB(int k) { return (B_CPR == 16) ? ((k & 3) << 2) : (((k >> 1) & 1) << 2); }

  DI static void init_ptrs(const u16* __restrict__ A, int lda, const u16* __restrict__ B, int ldb, int tid,
                           const u16* (&pa)[A_GLDS], const u16* (&pb)[B_GLDS]) {
#pragma unroll
    for (int i = 0; i < A_GLDS; ++i) {
      const int ci = i * NTH + tid, row = ci >> 3, c = (ci & 7) ^ ((row >> 1) & 7);
      pa[i] = A + (size_t)row * lda + c * 8;
    }
#pragma unroll
    for (int i = 0; i < B_GLDS; ++i) {
      const int ci = i * NTH + tid, k = ci / B_CPR, c = (ci % B_CPR) ^ swzB(k);
      pb[i] = B + (size_t)k * ldb + c * 8;
    }
  }
  DI static void stage(char* buf, const u16* (&pa)[A_GLDS], const u16* (&pb)[B_GLDS], int ldb, int wbase) {
#pragma unroll
    for (int i = 0; i < A_GLDS; ++i) {
      glds16(pa[i], buf + i * (NTH * 16) + wbase);
      pa[i] += 64; asm volatile("" : "+v"(pa[i]));
    }
#pragma unroll
    for (int i = 0; i < B_GLDS; ++i) {
      glds16(pb[i], buf + A_BYTES + i * (NTH * 16) + wbase);
      pb[i] += (size_t)64 * ldb; asm volatile("" : "+v"(pb[i]));
    }
  }

  DI static void run(char* lds, const u16* __restrict__ A, int lda, const u16* __restrict__ B, int ldb,
                     f32x16 (&acc)[MT][NT], float& rsum, float& rsq, float (&csum)[NT]) {
    const int tid = threadIdx.x, lane = tid & 63;
    const int w = __builtin_amdgcn_readfirstlane(tid >> 6), wm = w / WN, wn = w % WN;
    const int r = lane & 31, hh = lane >> 5;
    const int blk = (lane >> 4) & 1, q = (lane & 15) >> 2, p = lane & 3;
#pragma unroll
    for (int mi = 0; mi < MT; ++mi)
#pragma unroll
      for (int ni = 0; ni < NT; ++ni)
#pragma unroll
        for (int i = 0; i < 16; ++i) acc[mi][ni][i] = 0.f;
    rsum = 0.f; rsq = 0.f;
#pragma unroll
    for (int ni = 0; ni < NT; ++ni) csum[ni] = 0.f;
    const unsigned ONES = 0x3f803f80u;
    float rs4[4] = {0.f, 0.f, 0.f, 0.f}, rq4[4] = {0.f, 0.f, 0.f, 0.f};
    unsigned ones = ONES; asm volatile("" : "+v"(ones));
    unsigned a_off[MT][4], b_off[NT];
#pragma unroll
    for (int mi = 0; mi < MT; ++mi) {
      const int row = (wm * MT + mi) * 32 + r;
#pragma unroll
      for (int s = 0; s < 4; ++s) a_off[mi][s] = row * 128 + (((2 * s + hh) ^ ((row >> 1) & 7)) << 4);
    }
#pragma unroll
    for (int ni = 0; ni < NT; ++ni) {
      const int cc = ((wn * NT + ni) * 32 + blk * 16 + 4 * p) >> 3, k1 = 8 * hh + q;
      b_off[ni] = A_BYTES + k1 * (BN * 2) + ((cc ^ swzB(k1)) << 4) + (p & 1) * 8;
    }

    const u16* pa[A_GLDS]; const u16* pb[B_GLDS];
    init_ptrs(A, lda, B, ldb, tid, pa, pb);
    const int wbase = w * 1024;
#pragma unroll
    for (int kt = 0; kt < NS - 1; ++kt) stage(lds + kt * STAGE_BYTES, pa, pb, ldb, wbase);
#pragma unroll
    for (int kt = 0; kt < NK; ++kt) {
      {
        const int last = (kt + NS - 2 < NK - 1) ? (kt + NS - 2) : (NK - 1);
        wait_vm((last - kt) * NG);
      }
      bar();
      if (kt + NS - 1 < NK) stage(lds + ((kt + NS - 1) % NS) * STAGE_BYTES, pa, pb, ldb, wbase);
      {
        const char* sb = lds + (kt % NS) * STAGE_BYTES;
#pragma unroll
        for (int s = 0; s < 4; ++s) {
          bf16x8 af[MT], bfr[NT];
#pragma unroll
          for (int mi = 0; mi < MT; ++mi) af[mi] = *(const bf16x8*)(sb + a_off[mi][s]);
#pragma unroll
          for (int ni = 0; ni < NT; ++ni) {
            s16x4 lo = tr_read(sb + b_off[ni] + s * 16 * BN * 2);
            s16x4 hi = tr_read(sb + b_off[ni] + s * 16 * BN * 2 + 4 * BN * 2);
            bfr[ni] = __builtin_shufflevector(lo, hi, 0, 1, 2, 3, 4, 5, 6, 7);
          }
#pragma unroll
          for (int mi = 0; mi < MT; ++mi)
#pragma unroll
            for (int ni = 0; ni < NT; ++ni)
              acc[mi][ni] = SWAP ? MFMA32(bfr[ni], af[mi], acc[mi][ni]) : MFMA32(af[mi], bfr[ni], acc[mi][ni]);
          if (STATS) {
            u32x4 u = __builtin_bit_cast(u32x4, af[0]);
#pragma unroll
            for (int mi = 1; mi < MT; ++mi) if (wn == mi) u = __builtin_bit_cast(u32x4, af[mi]);
#pragma unroll
            for (int j = 0; j < 4; ++j) { dot2v(rs4[j], u[j], ones); dot2v(rq4[j], u[j], u[j]); }
          }
        }
        if (STATS) {
#pragma unroll
          for (int ni = 0; ni < NT; ++ni) {
            s16x4 lo = tr_read(sb + b_off[ni] + wm * 16 * BN * 2);
            s16x4 hi = tr_read(sb + b_off[ni] + wm * 16 * BN * 2 + 4 * BN * 2);
            u32x2 u0 = __builtin_bit_cast(u32x2, lo), u1 = __builtin_bit_cast(u32x2, hi);
            dot2v(csum[ni], u0[0], ones); dot2v(csum[ni], u0[1], ones); dot2v(csum[ni], u1[0], ones); dot2v(csum[ni], u1[1], ones);
          }
        }
      }
    }
    rsum = (rs4[0] + rs4[1]) + (rs4[2] + rs4[3]); rsq = (rq4[0] + rq4[1]) + (rq4[2] + rq4[3]);
    bar();
  }
};

typedef Gemm<256, 128, 4, 2, 3, 10, true, true> G1s;
typedef Gemm<256, 128, 4, 2, 3, 10, false, true> G1n;
constexpr int EP_RS = 144;
constexpr int EP_WAVE = 64 * EP_RS;

__global__ __launch_bounds__(512, 2) void k1_qkv_h(const u16* __restrict__ x, const u16* __restrict__ wqkv, const u16* __restrict__ w1,
                                                   u16* __restrict__ qb, u16* __restrict__ kb, u16* __restrict__ vT, u16* __restrict__ hb) {
  constexpr int KID = 0; (void)KID;
  __shared__ __attribute__((aligned(16))) char lds[G1s::LDS_BYTES];
  int mt, nt;
  { const int xcd = blockIdx.x & 7, idx = blockIdx.x >> 3;
    const int j = (xcd & 3) * 27 + idx; mt = (xcd >> 2) * 4 + (j & 3); nt = j >> 2; }
  const int tid = threadIdx.x, lane = tid & 63, w = tid >> 6, wm = w >> 1, wn = w & 1, r = lane & 31, hh = lane >> 5;
  const u16* A = x + (size_t)mt * 256 * D_;
  const u16* B; int ldb;
  if (nt < 15) { B = wqkv + nt * 128; ldb = 3 * D_; } else { B = w1 + (nt - 15) * 128; ldb = FF_; }
  f32x16 acc[2][2]; float rsum, rsq, csum[2];
  const bool isV = (nt >= 10 && nt < 15);
  STAMP(0);
  if (isV) G1n::run(lds, A, D_, B, ldb, acc, rsum, rsq, csum);
  else     G1s::run(lds, A, D_, B, ldb, acc, rsum, rsq, csum);
  STAMP(1);
  float* st = (float*)(lds + 8 * EP_WAVE);
  {
    float s1 = rsum + __shfl_xor(rsum, 32), s2 = rsq + __shfl_xor(rsq, 32);
    float m = s1 * (1.f / D_), v = fmaxf(s2 * (1.f / D_) - m * m, 0.f);
    float c0 = csum[0] + __shfl_xor(csum[0], 32), c1 = csum[1] + __shfl_xor(csum[1], 32);
    if (hh == 0) {
      const int row = (wm * 2 + wn) * 32 + r;
      st[row] = m; st[256 + row] = rsqrtf(v + 1e-5f);
      st[512 + wm * 128 + wn * 64 + r] = c0; st[512 + wm * 128 + wn * 64 + 32 + r] = c1;
    }
  }
  bar();
  char* ep = lds + w * EP_WAVE;
  const int tok0 = mt * 256 + wm * 64, n0 = wn * 64;
  if (!isV) {
    u16* dst; int ldd, cbase; bool relu = false;
    if (nt < 5) { dst = qb; ldd = D_; cbase = nt * 128; }
    else if (nt < 10) { dst = kb; ldd = D_; cbase = (nt - 5) * 128; }
    else { dst = hb; ldd = FF_; cbase = (nt - 15) * 128; relu = true; }
    float mean[2], rstd[2];
#pragma unroll
    for (int mi = 0; mi < 2; ++mi) { mean[mi] = st[wm * 64 + mi * 32 + r]; rstd[mi] = st[256 + wm * 64 + mi * 32 + r]; }
#pragma unroll
    for (int ni = 0; ni < 2; ++ni) {
#pragma unroll
      for (int qd = 0; qd < 4; ++qd) {
        const float* cp = st + 512 + n0 + ni * 32 + 8 * qd + 4 * hh;
        f32x4 cs = *(const f32x4*)(cp) + *(const f32x4*)(cp + 128) + *(const f32x4*)(cp + 256) + *(const f32x4*)(cp + 384);
#pragma unroll
        for (int mi = 0; mi < 2; ++mi) {
          float v[4];
#pragma unroll
          for (int j = 0; j < 4; ++j) {
            float t = rstd[mi] * (acc[mi][ni][4 * qd + j] - mean[mi] * cs[j]);
            v[j] = relu ? fmaxf(t, 0.f) : t;
          }
          *(u32x2*)(ep + (mi * 32 + r) * EP_RS + (ni * 32 + 8 * qd + 4 * hh) * 2) = (u32x2){pk2(v[0], v[1]), pk2(v[2], v[3])};
        }
      }
    }
    wave_lds_sync();
    if (nt < 10) {
#pragma unroll
      for (int it = 0; it < 16; ++it) {
        const int row = it * 4 + (lane >> 4), grp = lane & 15;
        const int n = cbase + n0 + grp * 4, head = n / DH_, d = n - head * DH_;
        u32x2 v = *(const u32x2*)(ep + row * EP_RS + grp * 8);
        u16* o = dst + ((size_t)head * S_ + tok0 + row) * 32 + d;
        *(u32x2*)o = v;
        if (d == 16) { *(u32x2*)(o + 4) = (u32x2){0u, 0u}; *(u32x4*)(o + 8) = (u32x4){0u, 0u, 0u, 0u}; }
      }
    } else {
#pragma unroll
      for (int i = 0; i < 8; ++i) {
        const int row = i * 8 + (lane >> 3), ch = lane & 7;
        u32x4 v = *(const u32x4*)(ep + row * EP_RS + ch * 16);
        *(u32x4*)(dst + (size_t)(tok0 + row) * ldd + cbase + n0 + ch * 8) = v;
      }
    }
  } else {
    const int cbase = (nt - 10) * 128;
    float cs[2];
#pragma unroll
    for (int ni = 0; ni < 2; ++ni) {
      const float* cp = st + 512 + n0 + ni * 32 + r;
      cs[ni] = cp[0] + cp[128] + cp[256] + cp[384];
    }
#pragma unroll
    for (int mi = 0; mi < 2; ++mi) {
#pragma unroll
      for (int qd = 0; qd < 4; ++qd) {
        f32x4 mn = *(const f32x4*)(st + wm * 64 + mi * 32 + 8 * qd + 4 * hh);
        f32x4 rs = *(const f32x4*)(st + 256 + wm * 64 + mi * 32 + 8 * qd + 4 * hh);
#pragma unroll
        for (int ni = 0; ni < 2; ++ni) {
          float v[4];
#pragma unroll
          for (int j = 0; j < 4; ++j) v[j] = rs[j] * (acc[mi][ni][4 * qd + j] - mn[j] * cs[ni]);
          *(u32x2*)(ep + (ni * 32 + r) * EP_RS + (mi * 32 + 8 * qd + 4 * hh) * 2) = (u32x2){pk2(v[0], v[1]), pk2(v[2], v[3])};
        }
      }
    }
    wave_lds_sync();
#pragma unroll
    for (int i = 0; i < 8; ++i) {
      const int row = i * 8 + (lane >> 3), ch = lane & 7;
      u32x4 v = *(const u32x4*)(ep + row * EP_RS + ch * 16);
      *(u32x4*)(vT + (size_t)(cbase + n0 + row) * S_ + tok0 + ch * 8) = v;
    }
  }
  STAMP(2);
}

constexpr int ATT_BLOCKS = NH_ * (S_ / 128);
constexpr int KS_BYTES = 384 * 64, VS_STRIDE = 776;
constexpr int ATT_LDS = KS_BYTES + (DH_ + 1) * VS_STRIDE + 8;

__global__ __launch_bounds__(256, 2) void k2_attn(const u16* __restrict__ qb, const u16* __restrict__ kb, const u16* __restrict__ vT,
                                                  u16* __restrict__ ob) {
  constexpr int KID = 1; (void)KID;
  __shared__ __attribute__((aligned(16))) char lds[ATT_LDS];
  const int tid = threadIdx.x, lane = tid & 63, w = tid >> 6, r = lane & 31, hh = lane >> 5;
  const int ab = blockIdx.x;
  const int h = (ab >> 3) & 31, q0 = ((ab & 7) * 2 + (ab >> 8)) * 128;
  const int kstart = q0 - WIN_;
  STAMP(0);
  char* Ks = lds;
  char* Vs = lds + KS_BYTES;
  {
    const u16* kbase = kb + (size_t)h * S_ * 32;
#pragma unroll
    for (int it = 0; it < 6; ++it) {
      const int ci = it * 256 + tid, ki = ci >> 2, c = (ci & 3) ^ ((ki >> 2) & 3);
      int key = kstart + ki; key = key < 0 ? 0 : key;
      glds16(kbase + (size_t)key * 32 + c * 8, Ks + it * 4096 + w * 1024);
    }
  }
  u32x4 vr[4];
#pragma unroll
  for (int it = 0; it < 4; ++it) {
    const int idx = tid + it * 256, d = idx / 48, c = idx % 48, key0 = kstart + c * 8;
    vr[it] = (u32x4){0u, 0u, 0u, 0u};
    if (d == DH_) vr[it] = (u32x4){0x3f803f80u, 0x3f803f80u, 0x3f803f80u, 0x3f803f80u};
    else if (d < DH_ && key0 >= 0) vr[it] = *(const u32x4*)(vT + (size_t)(h * DH_ + d) * S_ + key0);
  }
  const int qw = q0 + 32 * w;
  bf16x8 qf0, qf1;
  {
    const u16* src = qb + ((size_t)h * S_ + qw + r) * 32 + 8 * hh;
    qf0 = *(const bf16x8*)(src); qf1 = *(const bf16x8*)(src + 16);
  }
#pragma unroll
  for (int it = 0; it < 4; ++it) {
    const int idx = tid + it * 256, d = idx / 48, c = idx % 48;
    if (d <= DH_) {
      char* dst = Vs + d * VS_STRIDE + c * 16;
      *(u32x2*)(dst) = (u32x2){vr[it][0], vr[it][1]};
      *(u32x2*)(dst + 8) = (u32x2){vr[it][2], vr[it][3]};
    }
  }
  wait_vm_t<0>();
  __syncthreads();
  STAMP(1);
  const float NEG = -__builtin_inff();
  const int t_lo = (qw >= WIN_) ? 0 : (WIN_ - qw) / 32;
  f32x16 zero;
#pragma unroll
  for (int i = 0; i < 16; ++i) zero[i] = 0.f;
  auto score_tile = [&](int t) -> f32x16 {
    const int row = 32 * (w + t) + r, sw = (row >> 2) & 3;
    bf16x8 k0 = *(const bf16x8*)(Ks + row * 64 + ((hh ^ sw) << 4));
    bf16x8 k1 = *(const bf16x8*)(Ks + row * 64 + (((2 + hh) ^ sw) << 4));
    f32x16 z = MFMA32(k0, qf0, zero);
    z = MFMA32(k1, qf1, z);
    if (t == 0) {
#pragma unroll
      for (int i = 0; i < 16; ++i) if ((i & 3) + 8 * (i >> 2) + 4 * hh <= r) z[i] = NEG;
    }
    if (t == 8) {
#pragma unroll
      for (int i = 0; i < 16; ++i) if ((i & 3) + 8 * (i >> 2) + 4 * hh > r) z[i] = NEG;
    }
    return z;
  };
  f32x16 sc[9];
  float mx = NEG;
#pragma unroll
  for (int t = 0; t < 9; ++t) {
    if (t >= t_lo) {
      sc[t] = score_tile(t);
#pragma unroll
      for (int i = 0; i < 16; i += 2) mx = fmaxf(mx, fmaxf(sc[t][i], sc[t][i + 1]));
    }
  }
  mx = fmaxf(mx, __shfl_xor(mx, 32));
  STAMP(2);
  const float C2 = 0.22360679774997896f * 1.4426950408889634f;
  const float mo = -mx * C2;
  f32x16 oacc = zero;
  const bool dval = r <= DH_;
  const char* vrow = Vs + (dval ? r : 0) * VS_STRIDE;
#pragma unroll
  for (int t = 0; t < 9; ++t) {
    if (t >= t_lo) {
      float pv[16];
#pragma unroll
      for (int i = 0; i < 16; ++i) pv[i] = __builtin_amdgcn_exp2f(fmaf(sc[t][i], C2, mo));
#pragma unroll
      for (int s = 0; s < 2; ++s) {
        u32x4 pp = {pk2(pv[8 * s], pv[8 * s + 1]), pk2(pv[8 * s + 2], pv[8 * s + 3]), pk2(pv[8 * s + 4], pv[8 * s + 5]), pk2(pv[8 * s + 6], pv[8 * s + 7])};
        const int kidx = 32 * (w + t) + 16 * s + 4 * hh;
        u32x2 v0 = *(const u32x2*)(vrow + kidx * 2);
        u32x2 v1 = *(const u32x2*)(vrow + kidx * 2 + 16);
        u32x4 vv = {v0[0], v0[1], v1[0], v1[1]};
        if (!dval) vv = (u32x4){0u, 0u, 0u, 0u};
        oacc = MFMA32(__builtin_bit_cast(bf16x8, vv), __builtin_bit_cast(bf16x8, pp), oacc);
      }
    }
  }
  float sum = __shfl(oacc[8], r + 32);
  STAMP(3);
  const float inv = 1.f / sum;
  u16* orow = ob + (size_t)(qw + r) * D_ + h * DH_ + 4 * hh;
#pragma unroll
  for (int qd = 0; qd < 3; ++qd) {
    if (qd < 2 || hh == 0) {
      u32x2 o = {pk2(oacc[4 * qd] * inv, oacc[4 * qd + 1] * inv), pk2(oacc[4 * qd + 2] * inv, oacc[4 * qd + 3] * inv)};
      *(u32x2*)(orow + 8 * qd) = o;
    }
  }
}

typedef Gemm<128, 64, 2, 2, 4, 24, true, false> G3a;
typedef Gemm<128, 64, 2, 2, 4, 10, true, false> G3b;
constexpr int EP3_RS = 80;

__global__ __launch_bounds__(256, 1) void k3_out(const u16* __restrict__ ob, const u16* __restrict__ wo, const u16* __restrict__ hb,
                                                 const u16* __restrict__ w2, const u16* __restrict__ x, u16* __restrict__ out) {
  constexpr int KID = 2; (void)KID;
  __shared__ __attribute__((aligned(16))) char lds[G3a::LDS_BYTES];
  const int tid = threadIdx.x, lane = tid & 63, w = tid >> 6, r = lane & 31, hh = lane >> 5, wm = w >> 1, wn = w & 1;
  const int xcd = blockIdx.x & 7, idx = blockIdx.x >> 3;
  const int mt = xcd * 2 + idx / 10, nt = idx % 10;
  STAMP(0);
  u32x4 xv[4];
#pragma unroll
  for (int i = 0; i < 4; ++i)
    xv[i] = *(const u32x4*)(x + (size_t)(mt * 128 + wm * 64 + i * 16 + (lane >> 2)) * D_ + nt * 64 + wn * 32 + (lane & 3) * 8);
  f32x16 accg[2][1], accs[2][1]; float d0, d1, d2[1];
  G3a::run(lds, hb + (size_t)mt * 128 * FF_, FF_, w2 + nt * 64, D_, accg, d0, d1, d2);
  STAMP(1);
  G3b::run(lds, ob + (size_t)mt * 128 * D_, D_, wo + nt * 64, D_, accs, d0, d1, d2);
  STAMP(2);
  char* ep = lds + w * (64 * EP3_RS);
#pragma unroll
  for (int mi = 0; mi < 2; ++mi)
#pragma unroll
    for (int qd = 0; qd < 4; ++qd) {
      float o[4];
#pragma unroll
      for (int j = 0; j < 4; ++j) {
        const float g = rbf(accg[mi][0][4 * qd + j]);
        const float sg = rbf(g / (1.f + __expf(-g)));
        o[j] = rbf(accs[mi][0][4 * qd + j]) * sg;
      }
      *(u32x2*)(ep + (mi * 32 + r) * EP3_RS + (8 * qd + 4 * hh) * 2) = (u32x2){pk2(o[0], o[1]), pk2(o[2], o[3])};
    }
  wave_lds_sync();
#pragma unroll
  for (int i = 0; i < 4; ++i) {
    const int row = i * 16 + (lane >> 2), ch = lane & 3;
    u32x4 pv = *(const u32x4*)(ep + row * EP3_RS + ch * 16);
    float o[8];
#pragma unroll
    for (int j = 0; j < 8; ++j)
      o[j] = bf2f((u16)(xv[i][j >> 1] >> (16 * (j & 1)))) + bf2f((u16)(pv[j >> 1] >> (16 * (j & 1))));
    u32x4 ov = {pk2(o[0], o[1]), pk2(o[2], o[3]), pk2(o[4], o[5]), pk2(o[6], o[7])};
    *(u32x4*)(out + (size_t)(mt * 128 + wm * 64 + row) * D_ + nt * 64 + wn * 32 + ch * 8) = ov;
  }
  STAMP(3);
}

extern "C" void kernel_launch(void* const* d_in, const int* in_sizes, int n_in,
                              void* d_out, int out_size, void* d_ws, size_t ws_size,
                              hipStream_t stream) {
  const u16* x = (const u16*)d_in[0];
  const u16* wqkv = (const u16*)d_in[1];
  const u16* wo = (const u16*)d_in[2];
  const u16* w1 = (const u16*)d_in[4];
  const u16* w2 = (const u16*)d_in[5];
  u16* out = (u16*)d_out;
  char* ws = (char*)d_ws;
  const size_t SD = (size_t)S_ * D_ * 2;
  const size_t QP = (size_t)NH_ * S_ * 32 * 2;
  u16* qb = (u16*)(ws);
  u16* kb = (u16*)(ws + QP);
  u16* vT = (u16*)(ws + 2 * QP);
  u16* ob = (u16*)(ws + 2 * QP + SD);
  u16* hb = (u16*)(ws + 2 * QP + 2 * SD);
  k1_qkv_h<<<8 * 27, 512, 0, stream>>>(x, wqkv, w1, qb, kb, vT, hb);
  k2_attn<<<ATT_BLOCKS, 256, 0, stream>>>(qb, kb, vT, ob);
  k3_out<<<(S_ / 128) * (D_ / 64), 256, 0, stream>>>(ob, wo, hb, w2, x, out);
}
```

```cpp
#include <hip/hip_runtime.h>
#include <hip/hip_bf16.h>
#include <stdint.h>

typedef short bf16x8 __attribute__((ext_vector_type(8)));
typedef short s16x4 __attribute__((ext_vector_type(4)));
typedef __bf16 bf16x2_t __attribute__((ext_vector_type(2)));
typedef __bf16 bf16x4_t __attribute__((ext_vector_type(4)));
typedef float f32x16 __attribute__((ext_vector_type(16)));
typedef float f32x4 __attribute__((ext_vector_type(4)));
typedef unsigned u32x4 __attribute__((ext_vector_type(4)));
typedef unsigned u32x2 __attribute__((ext_vector_type(2)));
typedef unsigned short u16;

#define DI __device__ __forceinline__
#define MFMA32(a, b, c) __builtin_amdgcn_mfma_f32_32x32x16_bf16((a), (b), (c), 0, 0, 0)

#define STAMP(i) do { } while (0)

constexpr int S_ = 2048, D_ = 640, FF_ = 1536, NH_ = 32, DH_ = 20, WIN_ = 256;

template <int N> DI void wait_vm_t() { asm volatile("s_waitcnt vmcnt(%0)" ::"n"(N) : "memory"); }
DI void wait_vm(int n) {
  switch (n) {
    case 0: wait_vm_t<0>(); break;   case 2: wait_vm_t<2>(); break;   case 3: wait_vm_t<3>(); break;
    case 4: wait_vm_t<4>(); break;   case 6: wait_vm_t<6>(); break;   case 8: wait_vm_t<8>(); break;
    case 9: wait_vm_t<9>(); break;   case 12: wait_vm_t<12>(); break; case 16: wait_vm_t<16>(); break;
    case 18: wait_vm_t<18>(); break; case 24: wait_vm_t<24>(); break; case 30: wait_vm_t<30>(); break;
    default: wait_vm_t<0>(); break;
  }
}
template <int N> DI void wait_lgkm() { asm volatile("s_waitcnt lgkmcnt(%0)" ::"n"(N) : "memory"); }
DI void bar() {
  asm volatile("s_waitcnt lgkmcnt(0)" ::: "memory");
  __builtin_amdgcn_s_barrier();
  asm volatile("" ::: "memory");
}
DI void wave_lds_sync() { asm volatile("s_waitcnt lgkmcnt(0)" ::: "memory"); __builtin_amdgcn_wave_barrier(); }
DI u16 f2bf(float f) { return __builtin_bit_cast(u16, (__bf16)f); }
DI float bf2f(u16 v) { return __builtin_bit_cast(float, (unsigned)v << 16); }
DI float rbf(float f) { return bf2f(f2bf(f)); }
DI unsigned pk2(float a, float b) { return (unsigned)f2bf(a) | ((unsigned)f2bf(b) << 16); }
DI float dot2acc(unsigned a, unsigned b, float c) {
  return __builtin_amdgcn_fdot2_f32_bf16(__builtin_bit_cast(bf16x2_t, a), __builtin_bit_cast(bf16x2_t, b), c, false);
}
DI void dot2v(float& acc, unsigned a, unsigned b) { asm volatile("v_dot2c_f32_bf16 %0, %1, %2" : "+v"(acc) : "v"(a), "v"(b)); }
DI s16x4 tr_read(const char* p) {
  bf16x4_t v = __builtin_amdgcn_ds_read_tr16_b64_v4bf16((__attribute__((address_space(3))) bf16x4_t*)(p));
  return __builtin_bit_cast(s16x4, v);
}
DI void glds16(const void* g, char* l) {
  __builtin_amdgcn_global_load_lds((const __attribute__((address_space(1))) void*)g,
                                   (__attribute__((address_space(3))) void*)l, 16, 0, 0);
}

template <int BM, int BN, int WM, int WN, int NS, int NK, bool SWAP, bool STATS>
struct Gemm {
  static constexpr int NTH = WM * WN * 64;
  static constexpr int MT = BM / WM / 32, NT = BN / WN / 32;
  static constexpr int A_BYTES = BM * 128, B_BYTES = 64 * BN * 2, STAGE_BYTES = A_BYTES + B_BYTES;
  static constexpr int A_GLDS = BM * 8 / NTH, B_CPR = BN / 8, B_GLDS = 64 * B_CPR / NTH;
  static constexpr int NG = A_GLDS + B_GLDS;
  static constexpr int LDS_BYTES = NS * STAGE_BYTES;

  DI static int swzB(int k) { return (B_CPR == 16) ? ((k & 3) << 2) : (((k >> 1) & 1) << 2); }

  DI static void init_ptrs(const u16* __restrict__ A, int lda, const u16* __restrict__ B, int ldb, int tid,
                           const u16* (&pa)[A_GLDS], const u16* (&pb)[B_GLDS]) {
#pragma unroll
    for (int i = 0; i < A_GLDS; ++i) {
      const int ci = i * NTH + tid, row = ci >> 3, c = (ci & 7) ^ ((row >> 1) & 7);
      pa[i] = A + (size_t)row * lda + c * 8;
    }
#pragma unroll
    for (int i = 0; i < B_GLDS; ++i) {
      const int ci = i * NTH + tid, k = ci / B_CPR, c = (ci % B_CPR) ^ swzB(k);
      pb[i] = B + (size_t)k * ldb + c * 8;
    }
  }
  DI static void stage(char* buf, const u16* (&pa)[A_GLDS], const u16* (&pb)[B_GLDS], int ldb, int wbase) {
#pragma unroll
    for (int i = 0; i < A_GLDS; ++i) {
      glds16(pa[i], buf + i * (NTH * 16) + wbase);
      pa[i] += 64; asm volatile("" : "+v"(pa[i]));
    }
#pragma unroll
    for (int i = 0; i < B_GLDS; ++i) {
      glds16(pb[i], buf + A_BYTES + i * (NTH * 16) + wbase);
      pb[i] += (size_t)64 * ldb; asm volatile("" : "+v"(pb[i]));
    }
  }

  DI static void run(char* lds, const u16* __restrict__ A, int lda, const u16* __restrict__ B, int ldb,
                     f32x16 (&acc)[MT][NT], float& rsum, float& rsq, float (&csum)[NT]) {
    const int tid = threadIdx.x, lane = tid & 63;
    const int w = __builtin_amdgcn_readfirstlane(tid >> 6), wm = w / WN, wn = w % WN;
    const int r = lane & 31, hh = lane >> 5;
    const int blk = (lane >> 4) & 1, q = (lane & 15) >> 2, p = lane & 3;
#pragma unroll
    for (int mi = 0; mi < MT; ++mi)
#pragma unroll
      for (int ni = 0; ni < NT; ++ni)
#pragma unroll
        for (int i = 0; i < 16; ++i) acc[mi][ni][i] = 0.f;
    rsum = 0.f; rsq = 0.f;
#pragma unroll
    for (int ni = 0; ni < NT; ++ni) csum[ni] = 0.f;
    const unsigned ONES = 0x3f803f80u;
    float rs4[4] = {0.f, 0.f, 0.f, 0.f}, rq4[4] = {0.f, 0.f, 0.f, 0.f};
    unsigned ones = ONES; asm volatile("" : "+v"(ones));
    unsigned a_off[MT][4], b_off[NT];
#pragma unroll
    for (int mi = 0; mi < MT; ++mi) {
      const int row = (wm * MT + mi) * 32 + r;
#pragma unroll
      for (int s = 0; s < 4; ++s) a_off[mi][s] = row * 128 + (((2 * s + hh) ^ ((row >> 1) & 7)) << 4);
    }
#pragma unroll
    for (int ni = 0; ni < NT; ++ni) {
      const int cc = ((wn * NT + ni) * 32 + blk * 16 + 4 * p) >> 3, k1 = 8 * hh + q;
      b_off[ni] = A_BYTES + k1 * (BN * 2) + ((cc ^ swzB(k1)) << 4) + (p & 1) * 8;
    }

    const u16* pa[A_GLDS]; const u16* pb[B_GLDS];
    init_ptrs(A, lda, B, ldb, tid, pa, pb);
    const int wbase = w * 1024;
#pragma unroll
    for (int kt = 0; kt < NS - 1; ++kt) stage(lds + kt * STAGE_BYTES, pa, pb, ldb, wbase);
#pragma unroll
    for (int kt = 0; kt < NK; ++kt) {
      {
        const int last = (kt + NS - 2 < NK - 1) ? (kt + NS - 2) : (NK - 1);
        wait_vm((last - kt) * NG);
      }
      bar();
      if (kt + NS - 1 < NK) stage(lds + ((kt + NS - 1) % NS) * STAGE_BYTES, pa, pb, ldb, wbase);
      {
        const char* sb = lds + (kt % NS) * STAGE_BYTES;
#pragma unroll
        for (int s = 0; s < 4; ++s) {
          bf16x8 af[MT], bfr[NT];
#pragma unroll
          for (int mi = 0; mi < MT; ++mi) af[mi] = *(const bf16x8*)(sb + a_off[mi][s]);
#pragma unroll
          for (int ni = 0; ni < NT; ++ni) {
            s16x4 lo = tr_read(sb + b_off[ni] + s * 16 * BN * 2);
            s16x4 hi = tr_read(sb + b_off[ni] + s * 16 * BN * 2 + 4 * BN * 2);
            bfr[ni] = __builtin_shufflevector(lo, hi, 0, 1, 2, 3, 4, 5, 6, 7);
          }
#pragma unroll
          for (int mi = 0; mi < MT; ++mi)
#pragma unroll
            for (int ni = 0; ni < NT; ++ni)
              acc[mi][ni] = SWAP ? MFMA32(bfr[ni], af[mi], acc[mi][ni]) : MFMA32(af[mi], bfr[ni], acc[mi][ni]);
          if (STATS) {
            u32x4 u = __builtin_bit_cast(u32x4, af[0]);
#pragma unroll
            for (int mi = 1; mi < MT; ++mi) if (wn == mi) u = __builtin_bit_cast(u32x4, af[mi]);
#pragma unroll
            for (int j = 0; j < 4; ++j) { dot2v(rs4[j], u[j], ones); dot2v(rq4[j], u[j], u[j]); }
          }
        }
        if (STATS) {
#pragma unroll
          for (int ni = 0; ni < NT; ++ni) {
            s16x4 lo = tr_read(sb + b_off[ni] + wm * 16 * BN * 2);
            s16x4 hi = tr_read(sb + b_off[ni] + wm * 16 * BN * 2 + 4 * BN * 2);
            u32x2 u0 = __builtin_bit_cast(u32x2, lo), u1 = __builtin_bit_cast(u32x2, hi);
            dot2v(csum[ni], u0[0], ones); dot2v(csum[ni], u0[1], ones); dot2v(csum[ni], u1[0], ones); dot2v(csum[ni], u1[1], ones);
          }
        }
      }
    }
    rsum = (rs4[0] + rs4[1]) + (rs4[2] + rs4[3]); rsq = (rq4[0] + rq4[1]) + (rq4[2] + rq4[3]);
    bar();
  }
};

typedef Gemm<256, 128, 4, 2, 3, 10, true, true> G1s;
typedef Gemm<256, 128, 4, 2, 3, 10, false, true> G1n;
constexpr int EP_RS = 144;
constexpr int EP_WAVE = 64 * EP_RS;

__global__ __launch_bounds__(512, 2) void k1_qkv_h(const u16* __restrict__ x, const u16* __restrict__ wqkv, const u16* __restrict__ w1,
                                                   u16* __restrict__ qb, u16* __restrict__ kb, u16* __restrict__ vT, u16* __restrict__ hb) {
  constexpr int KID = 0; (void)KID;
  __shared__ __attribute__((aligned(16))) char lds[G1s::LDS_BYTES];
  int mt, nt;
  { const int xcd = blockIdx.x & 7, idx = blockIdx.x >> 3;
    const int j = (xcd & 3) * 27 + idx; mt = (xcd >> 2) * 4 + (j & 3); nt = j >> 2; }
  const int tid = threadIdx.x, lane = tid & 63, w = tid >> 6, wm = w >> 1, wn = w & 1, r = lane & 31, hh = lane >> 5;
  const u16* A = x + (size_t)mt * 256 * D_;
  const u16* B; int ldb;
  if (nt < 15) { B = wqkv + nt * 128; ldb = 3 * D_; } else { B = w1 + (nt - 15) * 128; ldb = FF_; }
  f32x16 acc[2][2]; float rsum, rsq, csum[2];
  const bool isV = (nt >= 10 && nt < 15);
  STAMP(0);
  if (isV) G1n::run(lds, A, D_, B, ldb, acc, rsum, rsq, csum);
  else     G1s::run(lds, A, D_, B, ldb, acc, rsum, rsq, csum);
  STAMP(1);
  float* st = (float*)(lds + 8 * EP_WAVE);
  {
    float s1 = rsum + __shfl_xor(rsum, 32), s2 = rsq + __shfl_xor(rsq, 32);
    float m = s1 * (1.f / D_), v = fmaxf(s2 * (1.f / D_) - m * m, 0.f);
    float c0 = csum[0] + __shfl_xor(csum[0], 32), c1 = csum[1] + __shfl_xor(csum[1], 32);
    if (hh == 0) {
      const int row = (wm * 2 + wn) * 32 + r;
      st[row] = m; st[256 + row] = rsqrtf(v + 1e-5f);
      st[512 + wm * 128 + wn * 64 + r] = c0; st[512 + wm * 128 + wn * 64 + 32 + r] = c1;
    }
  }
  bar();
  char* ep = lds + w * EP_WAVE;
  const int tok0 = mt * 256 + wm * 64, n0 = wn * 64;
  if (!isV) {
    u16* dst; int ldd, cbase; bool relu = false;
    if (nt < 5) { dst = qb; ldd = D_; cbase = nt * 128; }
    else if (nt < 10) { dst = kb; ldd = D_; cbase = (nt - 5) * 128; }
    else { dst = hb; ldd = FF_; cbase = (nt - 15) * 128; relu = true; }
    float mean[2], rstd[2];
#pragma unroll
    for (int mi = 0; mi < 2; ++mi) { mean[mi] = st[wm * 64 + mi * 32 + r]; rstd[mi] = st[256 + wm * 64 + mi * 32 + r]; }
#pragma unroll
    for (int ni = 0; ni < 2; ++ni) {
#pragma unroll
      for (int qd = 0; qd < 4; ++qd) {
        const float* cp = st + 512 + n0 + ni * 32 + 8 * qd + 4 * hh;
        f32x4 cs = *(const f32x4*)(cp) + *(const f32x4*)(cp + 128) + *(const f32x4*)(cp + 256) + *(const f32x4*)(cp + 384);
#pragma unroll
        for (int mi = 0; mi < 2; ++mi) {
          float v[4];
#pragma unroll
          for (int j = 0; j < 4; ++j) {
            float t = rstd[mi] * (acc[mi][ni][4 * qd + j] - mean[mi] * cs[j]);
            v[j] = relu ? fmaxf(t, 0.f) : t;
          }
          *(u32x2*)(ep + (mi * 32 + r) * EP_RS + (ni * 32 + 8 * qd + 4 * hh) * 2) = (u32x2){pk2(v[0], v[1]), pk2(v[2], v[3])};
        }
      }
    }
    wave_lds_sync();
#pragma unroll
    for (int i = 0; i < 8; ++i) {
      const int row = i * 8 + (lane >> 3), ch = lane & 7;
      u32x4 v = *(const u32x4*)(ep + row * EP_RS + ch * 16);
      *(u32x4*)(dst + (size_t)(tok0 + row) * ldd + cbase + n0 + ch * 8) = v;
    }
  } else {
    const int cbase = (nt - 10) * 128;
    float cs[2];
#pragma unroll
    for (int ni = 0; ni < 2; ++ni) {
      const float* cp = st + 512 + n0 + ni * 32 + r;
      cs[ni] = cp[0] + cp[128] + cp[256] + cp[384];
    }
#pragma unroll
    for (int mi = 0; mi < 2; ++mi) {
#pragma unroll
      for (int qd = 0; qd < 4; ++qd) {
        f32x4 mn = *(const f32x4*)(st + wm * 64 + mi * 32 + 8 * qd + 4 * hh);
        f32x4 rs = *(const f32x4*)(st + 256 + wm * 64 + mi * 32 + 8 * qd + 4 * hh);
#pragma unroll
        for (int ni = 0; ni < 2; ++ni) {
          float v[4];
#pragma unroll
          for (int j = 0; j < 4; ++j) v[j] = rs[j] * (acc[mi][ni][4 * qd + j] - mn[j] * cs[ni]);
          *(u32x2*)(ep + (ni * 32 + r) * EP_RS + (mi * 32 + 8 * qd + 4 * hh) * 2) = (u32x2){pk2(v[0], v[1]), pk2(v[2], v[3])};
        }
      }
    }
    wave_lds_sync();
#pragma unroll
    for (int i = 0; i < 8; ++i) {
      const int row = i * 8 + (lane >> 3), ch = lane & 7;
      u32x4 v = *(const u32x4*)(ep + row * EP_RS + ch * 16);
      *(u32x4*)(vT + (size_t)(cbase + n0 + row) * S_ + tok0 + ch * 8) = v;
    }
  }
  STAMP(2);
}

typedef Gemm<128, 64, 2, 2, 3, 24, true, false> G2;
constexpr int G2_BLOCKS = (S_ / 128) * (D_ / 64);
constexpr int ATT_BLOCKS = NH_ * (S_ / 256);
constexpr int KS_BYTES = 512 * 64, VS_STRIDE = 1032;
constexpr int EP2_RS = 80;
static_assert(KS_BYTES + (DH_ + 1) * VS_STRIDE <= G2::LDS_BYTES, "lds");

__global__ __launch_bounds__(256, 2) void k2_attn_w2(const u16* __restrict__ qb, const u16* __restrict__ kb, const u16* __restrict__ vT,
                                                     const u16* __restrict__ hb, const u16* __restrict__ w2,
                                                     u16* __restrict__ ob, u16* __restrict__ gb) {
  constexpr int KID = 1; (void)KID;
  __shared__ __attribute__((aligned(16))) char lds[G2::LDS_BYTES];
  const int tid = threadIdx.x, lane = tid & 63, w = tid >> 6, r = lane & 31, hh = lane >> 5;
  const int bid = blockIdx.x;
  STAMP(0);
  if (bid < G2_BLOCKS) {
    const int xcd = bid & 7, idx = bid >> 3;
    const int mt = xcd * 2 + idx / 10, nt = idx % 10;
    f32x16 acc[2][1]; float d0, d1, d2[1];
    G2::run(lds, hb + (size_t)mt * 128 * FF_, FF_, w2 + nt * 64, D_, acc, d0, d1, d2);
    const int wm = w >> 1, wn = w & 1;
    char* ep = lds + w * (64 * EP2_RS);
#pragma unroll
    for (int mi = 0; mi < 2; ++mi)
#pragma unroll
      for (int qd = 0; qd < 4; ++qd)
        *(u32x2*)(ep + (mi * 32 + r) * EP2_RS + (8 * qd + 4 * hh) * 2) = (u32x2){pk2(acc[mi][0][4 * qd], acc[mi][0][4 * qd + 1]), pk2(acc[mi][0][4 * qd + 2], acc[mi][0][4 * qd + 3])};
    wave_lds_sync();
#pragma unroll
    for (int i = 0; i < 4; ++i) {
      const int row = i * 16 + (lane >> 2), ch = lane & 3;
      u32x4 v = *(const u32x4*)(ep + row * EP2_RS + ch * 16);
      *(u32x4*)(gb + (size_t)(mt * 128 + wm * 64 + row) * D_ + nt * 64 + wn * 32 + ch * 8) = v;
    }
    STAMP(1); STAMP(2); STAMP(3);
    return;
  }
  const int ab = bid - G2_BLOCKS;
  const int h = ab >> 3, q0 = (ab & 7) * 256;
  const int kstart = q0 - WIN_;
  char* Ks = lds;
  char* Vs = lds + KS_BYTES;
  {
    u32x2 kr[2][5];
#pragma unroll
    for (int it = 0; it < 2; ++it) {
      const int key = kstart + tid + it * 256;
#pragma unroll
      for (int j = 0; j < 5; ++j) kr[it][j] = (u32x2){0u, 0u};
      if (key >= 0) {
        const u32x2* src = (const u32x2*)(kb + (size_t)key * D_ + h * DH_);
#pragma unroll
        for (int j = 0; j < 5; ++j) kr[it][j] = src[j];
      }
    }
    u32x4 vr[6];
#pragma unroll
    for (int it = 0; it < 6; ++it) {
      const int idx = tid + it * 256, d = idx >> 6, c = idx & 63, key0 = kstart + c * 8;
      vr[it] = (u32x4){0u, 0u, 0u, 0u};
      if (d == DH_) vr[it] = (u32x4){0x3f803f80u, 0x3f803f80u, 0x3f803f80u, 0x3f803f80u};
      else if (d < DH_ && key0 >= 0) vr[it] = *(const u32x4*)(vT + (size_t)(h * DH_ + d) * S_ + key0);
    }
#pragma unroll
    for (int it = 0; it < 2; ++it) {
      const int ki = tid + it * 256, sw = (ki >> 2) & 3;
      char* rowp = Ks + ki * 64;
      *(u32x4*)(rowp + ((0 ^ sw) << 4)) = (u32x4){kr[it][0][0], kr[it][0][1], kr[it][1][0], kr[it][1][1]};
      *(u32x4*)(rowp + ((1 ^ sw) << 4)) = (u32x4){kr[it][2][0], kr[it][2][1], kr[it][3][0], kr[it][3][1]};
      *(u32x4*)(rowp + ((2 ^ sw) << 4)) = (u32x4){kr[it][4][0], kr[it][4][1], 0u, 0u};
      *(u32x4*)(rowp + ((3 ^ sw) << 4)) = (u32x4){0u, 0u, 0u, 0u};
    }
#pragma unroll
    for (int it = 0; it < 6; ++it) {
      const int idx = tid + it * 256, d = idx >> 6, c = idx & 63;
      if (d <= DH_) {
        char* dst = Vs + d * VS_STRIDE + c * 16;
        *(u32x2*)(dst) = (u32x2){vr[it][0], vr[it][1]};
        *(u32x2*)(dst + 8) = (u32x2){vr[it][2], vr[it][3]};
      }
    }
  }
  bf16x8 qf0[2], qf1[2];
#pragma unroll
  for (int rep = 0; rep < 2; ++rep) {
    const u32x2* src = (const u32x2*)(qb + (size_t)(q0 + 32 * (w + 4 * rep) + r) * D_ + h * DH_);
    u32x2 a = src[2 * hh], b = src[2 * hh + 1], c = {0, 0};
    if (hh == 0) c = src[4];
    u32x4 f0 = {a[0], a[1], b[0], b[1]}, f1 = {c[0], c[1], 0u, 0u};
    qf0[rep] = __builtin_bit_cast(bf16x8, f0); qf1[rep] = __builtin_bit_cast(bf16x8, f1);
  }
  __syncthreads();
  STAMP(1);
  const float NEG = -__builtin_inff();
  f32x16 zero;
#pragma unroll
  for (int i = 0; i < 16; ++i) zero[i] = 0.f;
  const float C2 = 0.22360679774997896f * 1.4426950408889634f;
  const bool dval = r <= DH_;
  const char* vrow = Vs + (dval ? r : 0) * VS_STRIDE;
#pragma unroll
  for (int rep = 0; rep < 2; ++rep) {
    const int wq = w + 4 * rep, qw = q0 + 32 * wq;
    const int t_lo = (qw >= WIN_) ? 0 : (WIN_ - qw) / 32;
    f32x16 sc[9];
    float mx = NEG;
#pragma unroll
    for (int t = 0; t < 9; ++t) {
      if (t >= t_lo) {
        const int row = 32 * (wq + t) + r, sw = (row >> 2) & 3;
        bf16x8 k0 = *(const bf16x8*)(Ks + row * 64 + ((hh ^ sw) << 4));
        bf16x8 k1 = *(const bf16x8*)(Ks + row * 64 + (((2 + hh) ^ sw) << 4));
        f32x16 z = MFMA32(k0, qf0[rep], zero);
        z = MFMA32(k1, qf1[rep], z);
        if (t == 0) {
#pragma unroll
          for (int i = 0; i < 16; ++i) if ((i & 3) + 8 * (i >> 2) + 4 * hh <= r) z[i] = NEG;
        }
        if (t == 8) {
#pragma unroll
          for (int i = 0; i < 16; ++i) if ((i & 3) + 8 * (i >> 2) + 4 * hh > r) z[i] = NEG;
        }
        sc[t] = z;
#pragma unroll
        for (int i = 0; i < 16; i += 2) mx = fmaxf(mx, fmaxf(z[i], z[i + 1]));
      }
    }
    mx = fmaxf(mx, __shfl_xor(mx, 32));
    const float mo = -mx * C2;
    f32x16 oacc = zero;
#pragma unroll
    for (int t = 0; t < 9; ++t) {
      if (t >= t_lo) {
        float pv[16];
#pragma unroll
        for (int i = 0; i < 16; ++i) pv[i] = __builtin_amdgcn_exp2f(fmaf(sc[t][i], C2, mo));
#pragma unroll
        for (int s = 0; s < 2; ++s) {
          u32x4 pp = {pk2(pv[8 * s], pv[8 * s + 1]), pk2(pv[8 * s + 2], pv[8 * s + 3]), pk2(pv[8 * s + 4], pv[8 * s + 5]), pk2(pv[8 * s + 6], pv[8 * s + 7])};
          const int kidx = 32 * (wq + t) + 16 * s + 4 * hh;
          u32x2 v0 = *(const u32x2*)(vrow + kidx * 2);
          u32x2 v1 = *(const u32x2*)(vrow + kidx * 2 + 16);
          u32x4 vv = {v0[0], v0[1], v1[0], v1[1]};
          if (!dval) vv = (u32x4){0u, 0u, 0u, 0u};
          oacc = MFMA32(__builtin_bit_cast(bf16x8, vv), __builtin_bit_cast(bf16x8, pp), oacc);
        }
      }
    }
    const float inv = 1.f / __shfl(oacc[8], r + 32);
    u16* orow = ob + (size_t)(qw + r) * D_ + h * DH_ + 4 * hh;
#pragma unroll
    for (int qd = 0; qd < 3; ++qd) {
      if (qd < 2 || hh == 0) {
        u32x2 o = {pk2(oacc[4 * qd] * inv, oacc[4 * qd + 1] * inv), pk2(oacc[4 * qd + 2] * inv, oacc[4 * qd + 3] * inv)};
        *(u32x2*)(orow + 8 * qd) = o;
      }
    }
    if (rep == 0) STAMP(2);
  }
  STAMP(3);
}

typedef Gemm<64, 64, 2, 2, 4, 10, true, false> G3;

__global__ __launch_bounds__(256, 2) void k3_out(const u16* __restrict__ ob, const u16* __restrict__ wo, const u16* __restrict__ gb,
                                                 const u16* __restrict__ x, u16* __restrict__ out) {
  constexpr int KID = 2; (void)KID;
  __shared__ __attribute__((aligned(16))) char lds[G3::LDS_BYTES];
  const int tid = threadIdx.x, lane = tid & 63, w = tid >> 6, r = lane & 31, hh = lane >> 5, wm = w >> 1, wn = w & 1;
  const int xcd = blockIdx.x & 7, idx = blockIdx.x >> 3;
  const int mt = xcd * 4 + idx / 10, nt = idx % 10;
  STAMP(0);
  u32x4 gv[2], xv[2];
#pragma unroll
  for (int i = 0; i < 2; ++i) {
    const size_t off = (size_t)(mt * 64 + wm * 32 + i * 16 + (lane >> 2)) * D_ + nt * 64 + wn * 32 + (lane & 3) * 8;
    gv[i] = *(const u32x4*)(gb + off);
    xv[i] = *(const u32x4*)(x + off);
  }
  f32x16 acc[1][1]; float d0, d1, d2[1];
  G3::run(lds, ob + (size_t)mt * 64 * D_, D_, wo + nt * 64, D_, acc, d0, d1, d2);
  STAMP(1);
  char* ep = lds + w * (32 * EP2_RS);
#pragma unroll
  for (int qd = 0; qd < 4; ++qd)
    *(u32x2*)(ep + r * EP2_RS + (8 * qd + 4 * hh) * 2) = (u32x2){pk2(acc[0][0][4 * qd], acc[0][0][4 * qd + 1]), pk2(acc[0][0][4 * qd + 2], acc[0][0][4 * qd + 3])};
  wave_lds_sync();
#pragma unroll
  for (int i = 0; i < 2; ++i) {
    const int row = i * 16 + (lane >> 2), ch = lane & 3;
    u32x4 sv = *(const u32x4*)(ep + row * EP2_RS + ch * 16);
    float o[8];
#pragma unroll
    for (int j = 0; j < 8; ++j) {
      const float g = bf2f((u16)(gv[i][j >> 1] >> (16 * (j & 1))));
      const float xx = bf2f((u16)(xv[i][j >> 1] >> (16 * (j & 1))));
      const float sf = bf2f((u16)(sv[j >> 1] >> (16 * (j & 1))));
      const float sg = rbf(g / (1.f + __expf(-g)));
      o[j] = xx + rbf(sf * sg);
    }
    u32x4 ov = {pk2(o[0], o[1]), pk2(o[2], o[3]), pk2(o[4], o[5]), pk2(o[6], o[7])};
    *(u32x4*)(out + (size_t)(mt * 64 + wm * 32 + row) * D_ + nt * 64 + wn * 32 + ch * 8) = ov;
  }
  STAMP(2); STAMP(3);
}

extern "C" void kernel_launch(void* const* d_in, const int* in_sizes, int n_in,
                              void* d_out, int out_size, void* d_ws, size_t ws_size,
                              hipStream_t stream) {
  const u16* x = (const u16*)d_in[0];
  const u16* wqkv = (const u16*)d_in[1];
  const u16* wo = (const u16*)d_in[2];
  const u16* w1 = (const u16*)d_in[4];
  const u16* w2 = (const u16*)d_in[5];
  u16* out = (u16*)d_out;
  char* ws = (char*)d_ws;
  const size_t SD = (size_t)S_ * D_ * 2;
  u16* qb = (u16*)(ws);
  u16* kb = (u16*)(ws + SD);
  u16* vT = (u16*)(ws + 2 * SD);
  u16* ob = (u16*)(ws + 3 * SD);
  u16* gb = (u16*)(ws + 4 * SD);
  u16* hb = (u16*)(ws + 5 * SD);
  k1_qkv_h<<<8 * 27, 512, 0, stream>>>(x, wqkv, w1, qb, kb, vT, hb);
  k2_attn_w2<<<G2_BLOCKS + ATT_BLOCKS, 256, 0, stream>>>(qb, kb, vT, hb, w2, ob, gb);
  k3_out<<<(S_ / 64) * (D_ / 64), 256, 0, stream>>>(ob, wo, gb, x, out);
}
```

```cpp
#include <hip/hip_runtime.h>
#include <hip/hip_bf16.h>
#include <stdint.h>

typedef short bf16x8 __attribute__((ext_vector_type(8)));
typedef short s16x4 __attribute__((ext_vector_type(4)));
typedef __bf16 bf16x2_t __attribute__((ext_vector_type(2)));
typedef __bf16 bf16x4_t __attribute__((ext_vector_type(4)));
typedef float f32x16 __attribute__((ext_vector_type(16)));
typedef float f32x4 __attribute__((ext_vector_type(4)));
typedef unsigned u32x4 __attribute__((ext_vector_type(4)));
typedef unsigned u32x2 __attribute__((ext_vector_type(2)));
typedef unsigned short u16;

#define DI __device__ __forceinline__
#define MFMA32(a, b, c) __builtin_amdgcn_mfma_f32_32x32x16_bf16((a), (b), (c), 0, 0, 0)


constexpr int S_ = 2048, D_ = 640, FF_ = 1536, NH_ = 32, DH_ = 20, WIN_ = 256;

template <int N> DI void wait_vm_t() { asm volatile("s_waitcnt vmcnt(%0)" ::"n"(N) : "memory"); }
DI void wait_vm(int n) {
  switch (n) {
    case 0: wait_vm_t<0>(); break;   case 2: wait_vm_t<2>(); break;   case 3: wait_vm_t<3>(); break;
    case 4: wait_vm_t<4>(); break;   case 6: wait_vm_t<6>(); break;   case 8: wait_vm_t<8>(); break;
    case 9: wait_vm_t<9>(); break;   case 12: wait_vm_t<12>(); break; case 16: wait_vm_t<16>(); break;
    case 18: wait_vm_t<18>(); break; case 24: wait_vm_t<24>(); break; case 30: wait_vm_t<30>(); break;
    default: wait_vm_t<0>(); break;
  }
}
template <int N> DI void wait_lgkm() { asm volatile("s_waitcnt lgkmcnt(%0)" ::"n"(N) : "memory"); }
DI void bar() {
  asm volatile("s_waitcnt lgkmcnt(0)" ::: "memory");
  __builtin_amdgcn_s_barrier();
  asm volatile("" ::: "memory");
}
DI void wave_lds_sync() { asm volatile("s_waitcnt lgkmcnt(0)" ::: "memory"); __builtin_amdgcn_wave_barrier(); }
DI u16 f2bf(float f) { return __builtin_bit_cast(u16, (__bf16)f); }
DI float bf2f(u16 v) { return __builtin_bit_cast(float, (unsigned)v << 16); }
DI float rbf(float f) { return bf2f(f2bf(f)); }
typedef float f32x2 __attribute__((ext_vector_type(2)));
DI unsigned pk2(float a, float b) { f32x2 v = {a, b}; return __builtin_bit_cast(unsigned, __builtin_convertvector(v, bf16x2_t)); }
DI float max3f(float a, float b, float c) { float r; asm("v_max3_f32 %0, %1, %2, %3" : "=v"(r) : "v"(a), "v"(b), "v"(c)); return r; }
DI float dot2acc(unsigned a, unsigned b, float c) {
  return __builtin_amdgcn_fdot2_f32_bf16(__builtin_bit_cast(bf16x2_t, a), __builtin_bit_cast(bf16x2_t, b), c, false);
}
DI void dot2v(float& acc, unsigned a, unsigned b) { asm volatile("v_dot2c_f32_bf16 %0, %1, %2" : "+v"(acc) : "v"(a), "v"(b)); }
DI s16x4 tr_read(const char* p) {
  bf16x4_t v = __builtin_amdgcn_ds_read_tr16_b64_v4bf16((__attribute__((address_space(3))) bf16x4_t*)(p));
  return __builtin_bit_cast(s16x4, v);
}
DI void glds16(const void* g, char* l) {
  __builtin_amdgcn_global_load_lds((const __attribute__((address_space(1))) void*)g,
                                   (__attribute__((address_space(3))) void*)l, 16, 0, 0);
}

template <int BM, int BN, int WM, int WN, int NS, int NK, bool SWAP, bool STATS>
struct Gemm {
  static constexpr int NTH = WM * WN * 64;
  static constexpr int MT = BM / WM / 32, NT = BN / WN / 32;
  static constexpr int A_BYTES = BM * 128, B_BYTES = 64 * BN * 2, STAGE_BYTES = A_BYTES + B_BYTES;
  static constexpr int A_GLDS = BM * 8 / NTH, B_CPR = BN / 8, B_GLDS = 64 * B_CPR / NTH;
  static constexpr int NG = A_GLDS + B_GLDS;
  static constexpr int LDS_BYTES = NS * STAGE_BYTES;

  DI static int swzB(int k) { return (B_CPR == 16) ? ((k & 3) << 2) : (((k >> 1) & 1) << 2); }

  DI static void init_ptrs(const u16* __restrict__ A, int lda, const u16* __restrict__ B, int ldb, int tid,
                           const u16* (&pa)[A_GLDS], const u16* (&pb)[B_GLDS]) {
#pragma unroll
    for (int i = 0; i < A_GLDS; ++i) {
      const int ci = i * NTH + tid, row = ci >> 3, c = (ci & 7) ^ ((row >> 1) & 7);
      pa[i] = A + (size_t)row * lda + c * 8;
    }
#pragma unroll
    for (int i = 0; i < B_GLDS; ++i) {
      const int ci = i * NTH + tid, k = ci / B_CPR, c = (ci % B_CPR) ^ swzB(k);
      pb[i] = B + (size_t)k * ldb + c * 8;
    }
  }
  DI static void stage(char* buf, const u16* (&pa)[A_GLDS], const u16* (&pb)[B_GLDS], int ldb, int wbase) {
#pragma unroll
    for (int i = 0; i < A_GLDS; ++i) {
      glds16(pa[i], buf + i * (NTH * 16) + wbase);
      pa[i] += 64; asm volatile("" : "+v"(pa[i]));
    }
#pragma unroll
    for (int i = 0; i < B_GLDS; ++i) {
      glds16(pb[i], buf + A_BYTES + i * (NTH * 16) + wbase);
      pb[i] += (size_t)64 * ldb; asm volatile("" : "+v"(pb[i]));
    }
  }

  DI static void run(char* lds, const u16* __restrict__ A, int lda, const u16* __restrict__ B, int ldb,
                     f32x16 (&acc)[MT][NT], float& rsum, float& rsq, float (&csum)[NT]) {
    const int tid = threadIdx.x, lane = tid & 63;
    const int w = __builtin_amdgcn_readfirstlane(tid >> 6), wm = w / WN, wn = w % WN;
    const int r = lane & 31, hh = lane >> 5;
    const int blk = (lane >> 4) & 1, q = (lane & 15) >> 2, p = lane & 3;
#pragma unroll
    for (int mi = 0; mi < MT; ++mi)
#pragma unroll
      for (int ni = 0; ni < NT; ++ni)
#pragma unroll
        for (int i = 0; i < 16; ++i) acc[mi][ni][i] = 0.f;
    rsum = 0.f; rsq = 0.f;
#pragma unroll
    for (int ni = 0; ni < NT; ++ni) csum[ni] = 0.f;
    const unsigned ONES = 0x3f803f80u;
    float rs4[4] = {0.f, 0.f, 0.f, 0.f}, rq4[4] = {0.f, 0.f, 0.f, 0.f};
    unsigned ones = ONES; asm volatile("" : "+v"(ones));
    unsigned a_off[MT][4], b_off[NT];
#pragma unroll
    for (int mi = 0; mi < MT; ++mi) {
      const int row = (wm * MT + mi) * 32 + r;
#pragma unroll
      for (int s = 0; s < 4; ++s) a_off[mi][s] = row * 128 + (((2 * s + hh) ^ ((row >> 1) & 7)) << 4);
    }
#pragma unroll
    for (int ni = 0; ni < NT; ++ni) {
      const int cc = ((wn * NT + ni) * 32 + blk * 16 + 4 * p) >> 3, k1 = 8 * hh + q;
      b_off[ni] = A_BYTES + k1 * (BN * 2) + ((cc ^ swzB(k1)) << 4) + (p & 1) * 8;
    }

    const u16* pa[A_GLDS]; const u16* pb[B_GLDS];
    init_ptrs(A, lda, B, ldb, tid, pa, pb);
    const int wbase = w * 1024;
#pragma unroll
    for (int kt = 0; kt < NS - 1; ++kt) stage(lds + kt * STAGE_BYTES, pa, pb, ldb, wbase);
#pragma unroll
    for (int kt = 0; kt < NK; ++kt) {
      {
        const int last = (kt + NS - 2 < NK - 1) ? (kt + NS - 2) : (NK - 1);
        wait_vm((last - kt) * NG);
      }
      bar();
      if (kt + NS - 1 < NK) stage(lds + ((kt + NS - 1) % NS) * STAGE_BYTES, pa, pb, ldb, wbase);
      {
        const char* sb = lds + (kt % NS) * STAGE_BYTES;
#pragma unroll
        for (int s = 0; s < 4; ++s) {
          bf16x8 af[MT], bfr[NT];
#pragma unroll
          for (int mi = 0; mi < MT; ++mi) af[mi] = *(const bf16x8*)(sb + a_off[mi][s]);
#pragma unroll
          for (int ni = 0; ni < NT; ++ni) {
            s16x4 lo = tr_read(sb + b_off[ni] + s * 16 * BN * 2);
            s16x4 hi = tr_read(sb + b_off[ni] + s * 16 * BN * 2 + 4 * BN * 2);
            bfr[ni] = __builtin_shufflevector(lo, hi, 0, 1, 2, 3, 4, 5, 6, 7);
          }
#pragma unroll
          for (int mi = 0; mi < MT; ++mi)
#pragma unroll
            for (int ni = 0; ni < NT; ++ni)
              acc[mi][ni] = SWAP ? MFMA32(bfr[ni], af[mi], acc[mi][ni]) : MFMA32(af[mi], bfr[ni], acc[mi][ni]);
          if (STATS) {
            u32x4 u = __builtin_bit_cast(u32x4, af[0]);
#pragma unroll
            for (int mi = 1; mi < MT; ++mi) if (wn == mi) u = __builtin_bit_cast(u32x4, af[mi]);
#pragma unroll
            for (int j = 0; j < 4; ++j) { dot2v(rs4[j], u[j], ones); dot2v(rq4[j], u[j], u[j]); }
          }
        }
        if (STATS) {
#pragma unroll
          for (int ni = 0; ni < NT; ++ni) {
            s16x4 lo = tr_read(sb + b_off[ni] + wm * 16 * BN * 2);
            s16x4 hi = tr_read(sb + b_off[ni] + wm * 16 * BN * 2 + 4 * BN * 2);
            u32x2 u0 = __builtin_bit_cast(u32x2, lo), u1 = __builtin_bit_cast(u32x2, hi);
            dot2v(csum[ni], u0[0], ones); dot2v(csum[ni], u0[1], ones); dot2v(csum[ni], u1[0], ones); dot2v(csum[ni], u1[1], ones);
          }
        }
      }
    }
    rsum = (rs4[0] + rs4[1]) + (rs4[2] + rs4[3]); rsq = (rq4[0] + rq4[1]) + (rq4[2] + rq4[3]);
    bar();
  }
};

typedef Gemm<256, 128, 4, 2, 3, 10, true, true> G1s;
typedef Gemm<256, 128, 4, 2, 3, 10, false, true> G1n;
constexpr int EP_RS = 144;
constexpr int EP_WAVE = 64 * EP_RS;

__global__ __launch_bounds__(512, 2) void k1_qkv_h(const u16* __restrict__ x, const u16* __restrict__ wqkv, const u16* __restrict__ w1,
                                                   u16* __restrict__ qb, u16* __restrict__ kb, u16* __restrict__ vT, u16* __restrict__ hb) {
  __shared__ __attribute__((aligned(16))) char lds[G1s::LDS_BYTES];
  int mt, nt;
  { const int xcd = blockIdx.x & 7, idx = blockIdx.x >> 3;
    const int j = (xcd & 3) * 27 + idx; mt = (xcd >> 2) * 4 + (j & 3); nt = j >> 2; }
  const int tid = threadIdx.x, lane = tid & 63, w = tid >> 6, wm = w >> 1, wn = w & 1, r = lane & 31, hh = lane >> 5;
  const u16* A = x + (size_t)mt * 256 * D_;
  const u16* B; int ldb;
  if (nt < 15) { B = wqkv + nt * 128; ldb = 3 * D_; } else { B = w1 + (nt - 15) * 128; ldb = FF_; }
  f32x16 acc[2][2]; float rsum, rsq, csum[2];
  const bool isV = (nt >= 10 && nt < 15);
  if (isV) G1n::run(lds, A, D_, B, ldb, acc, rsum, rsq, csum);
  else     G1s::run(lds, A, D_, B, ldb, acc, rsum, rsq, csum);
  float* st = (float*)(lds + 8 * EP_WAVE);
  {
    float s1 = rsum + __shfl_xor(rsum, 32), s2 = rsq + __shfl_xor(rsq, 32);
    float m = s1 * (1.f / D_), v = fmaxf(s2 * (1.f / D_) - m * m, 0.f);
    float c0 = csum[0] + __shfl_xor(csum[0], 32), c1 = csum[1] + __shfl_xor(csum[1], 32);
    if (hh == 0) {
      const int row = (wm * 2 + wn) * 32 + r;
      st[row] = m; st[256 + row] = rsqrtf(v + 1e-5f);
      st[512 + wm * 128 + wn * 64 + r] = c0; st[512 + wm * 128 + wn * 64 + 32 + r] = c1;
    }
  }
  bar();
  char* ep = lds + w * EP_WAVE;
  const int tok0 = mt * 256 + wm * 64, n0 = wn * 64;
  if (!isV) {
    u16* dst; int ldd, cbase; bool relu = false;
    if (nt < 5) { dst = qb; ldd = D_; cbase = nt * 128; }
    else if (nt < 10) { dst = kb; ldd = D_; cbase = (nt - 5) * 128; }
    else { dst = hb; ldd = FF_; cbase = (nt - 15) * 128; relu = true; }
    float mean[2], rstd[2];
#pragma unroll
    for (int mi = 0; mi < 2; ++mi) { mean[mi] = st[wm * 64 + mi * 32 + r]; rstd[mi] = st[256 + wm * 64 + mi * 32 + r]; }
#pragma unroll
    for (int ni = 0; ni < 2; ++ni) {
#pragma unroll
      for (int qd = 0; qd < 4; ++qd) {
        const float* cp = st + 512 + n0 + ni * 32 + 8 * qd + 4 * hh;
        f32x4 cs = *(const f32x4*)(cp) + *(const f32x4*)(cp + 128) + *(const f32x4*)(cp + 256) + *(const f32x4*)(cp + 384);
#pragma unroll
        for (int mi = 0; mi < 2; ++mi) {
          float v[4];
#pragma unroll
          for (int j = 0; j < 4; ++j) {
            float t = rstd[mi] * (acc[mi][ni][4 * qd + j] - mean[mi] * cs[j]);
            v[j] = relu ? fmaxf(t, 0.f) : t;
          }
          *(u32x2*)(ep + (mi * 32 + r) * EP_RS + (ni * 32 + 8 * qd + 4 * hh) * 2) = (u32x2){pk2(v[0], v[1]), pk2(v[2], v[3])};
        }
      }
    }
    wave_lds_sync();
#pragma unroll
    for (int i = 0; i < 8; ++i) {
      const int row = i * 8 + (lane >> 3), ch = lane & 7;
      u32x4 v = *(const u32x4*)(ep + row * EP_RS + ch * 16);
      *(u32x4*)(dst + (size_t)(tok0 + row) * ldd + cbase + n0 + ch * 8) = v;
    }
  } else {
    const int cbase = (nt - 10) * 128;
    float cs[2];
#pragma unroll
    for (int ni = 0; ni < 2; ++ni) {
      const float* cp = st + 512 + n0 + ni * 32 + r;
      cs[ni] = cp[0] + cp[128] + cp[256] + cp[384];
    }
#pragma unroll
    for (int mi = 0; mi < 2; ++mi) {
#pragma unroll
      for (int qd = 0; qd < 4; ++qd) {
        f32x4 mn = *(const f32x4*)(st + wm * 64 + mi * 32 + 8 * qd + 4 * hh);
        f32x4 rs = *(const f32x4*)(st + 256 + wm * 64 + mi * 32 + 8 * qd + 4 * hh);
#pragma unroll
        for (int ni = 0; ni < 2; ++ni) {
          float v[4];
#pragma unroll
          for (int j = 0; j < 4; ++j) v[j] = rs[j] * (acc[mi][ni][4 * qd + j] - mn[j] * cs[ni]);
          *(u32x2*)(ep + (ni * 32 + r) * EP_RS + (mi * 32 + 8 * qd + 4 * hh) * 2) = (u32x2){pk2(v[0], v[1]), pk2(v[2], v[3])};
        }
      }
    }
    wave_lds_sync();
#pragma unroll
    for (int i = 0; i < 8; ++i) {
      const int row = i * 8 + (lane >> 3), ch = lane & 7;
      u32x4 v = *(const u32x4*)(ep + row * EP_RS + ch * 16);
      *(u32x4*)(vT + (size_t)(cbase + n0 + row) * S_ + tok0 + ch * 8) = v;
    }
  }
}

typedef Gemm<128, 64, 2, 2, 3, 24, true, false> G2;
constexpr int G2_BLOCKS = (S_ / 128) * (D_ / 64);
constexpr int ATT_BLOCKS = NH_ * (S_ / 256);
constexpr int KS_BYTES = 512 * 64, VS_STRIDE = 1032;
constexpr int EP2_RS = 80;
static_assert(KS_BYTES + (DH_ + 1) * VS_STRIDE <= G2::LDS_BYTES, "lds");

__global__ __launch_bounds__(256, 2) void k2_attn_w2(const u16* __restrict__ qb, const u16* __restrict__ kb, const u16* __restrict__ vT,
                                                     const u16* __restrict__ hb, const u16* __restrict__ w2,
                                                     u16* __restrict__ ob, u16* __restrict__ gb) {
  __shared__ __attribute__((aligned(16))) char lds[G2::LDS_BYTES];
  const int tid = threadIdx.x, lane = tid & 63, w = __builtin_amdgcn_readfirstlane(tid >> 6), r = lane & 31, hh = lane >> 5;
  const int bid = blockIdx.x;
  if (bid < G2_BLOCKS) {
    const int xcd = bid & 7, idx = bid >> 3;
    const int mt = xcd * 2 + idx / 10, nt = idx % 10;
    f32x16 acc[2][1]; float d0, d1, d2[1];
    G2::run(lds, hb + (size_t)mt * 128 * FF_, FF_, w2 + nt * 64, D_, acc, d0, d1, d2);
    const int wm = w >> 1, wn = w & 1;
    char* ep = lds + w * (64 * EP2_RS);
#pragma unroll
    for (int mi = 0; mi < 2; ++mi)
#pragma unroll
      for (int qd = 0; qd < 4; ++qd)
        *(u32x2*)(ep + (mi * 32 + r) * EP2_RS + (8 * qd + 4 * hh) * 2) = (u32x2){pk2(acc[mi][0][4 * qd], acc[mi][0][4 * qd + 1]), pk2(acc[mi][0][4 * qd + 2], acc[mi][0][4 * qd + 3])};
    wave_lds_sync();
#pragma unroll
    for (int i = 0; i < 4; ++i) {
      const int row = i * 16 + (lane >> 2), ch = lane & 3;
      u32x4 v = *(const u32x4*)(ep + row * EP2_RS + ch * 16);
      *(u32x4*)(gb + (size_t)(mt * 128 + wm * 64 + row) * D_ + nt * 64 + wn * 32 + ch * 8) = v;
    }
    return;
  }
  const int ab = bid - G2_BLOCKS;
  const int h = ab >> 3, q0 = (ab & 7) * 256;
  const int kstart = q0 - WIN_;
  char* Ks = lds;
  char* Vs = lds + KS_BYTES;
  {
    u32x2 kr[2][5];
#pragma unroll
    for (int it = 0; it < 2; ++it) {
      const int key = kstart + tid + it * 256;
#pragma unroll
      for (int j = 0; j < 5; ++j) kr[it][j] = (u32x2){0u, 0u};
      if (key >= 0) {
        const u32x2* src = (const u32x2*)(kb + (size_t)key * D_ + h * DH_);
#pragma unroll
        for (int j = 0; j < 5; ++j) kr[it][j] = src[j];
      }
    }
    u32x4 vr[6];
#pragma unroll
    for (int it = 0; it < 6; ++it) {
      const int idx = tid + it * 256, d = idx >> 6, c = idx & 63, key0 = kstart + c * 8;
      vr[it] = (u32x4){0u, 0u, 0u, 0u};
      if (d == DH_) vr[it] = (u32x4){0x3f803f80u, 0x3f803f80u, 0x3f803f80u, 0x3f803f80u};
      else if (d < DH_ && key0 >= 0) vr[it] = *(const u32x4*)(vT + (size_t)(h * DH_ + d) * S_ + key0);
    }
#pragma unroll
    for (int it = 0; it < 2; ++it) {
      const int ki = tid + it * 256, sw = (ki >> 2) & 3;
      char* rowp = Ks + ki * 64;
      *(u32x4*)(rowp + ((0 ^ sw) << 4)) = (u32x4){kr[it][0][0], kr[it][0][1], kr[it][1][0], kr[it][1][1]};
      *(u32x4*)(rowp + ((1 ^ sw) << 4)) = (u32x4){kr[it][2][0], kr[it][2][1], kr[it][3][0], kr[it][3][1]};
      *(u32x4*)(rowp + ((2 ^ sw) << 4)) = (u32x4){kr[it][4][0], kr[it][4][1], 0u, 0u};
      *(u32x4*)(rowp + ((3 ^ sw) << 4)) = (u32x4){0u, 0u, 0u, 0u};
    }
#pragma unroll
    for (int it = 0; it < 6; ++it) {
      const int idx = tid + it * 256, d = idx >> 6, c = idx & 63;
      if (d <= DH_) {
        char* dst = Vs + d * VS_STRIDE + c * 16;
        *(u32x2*)(dst) = (u32x2){vr[it][0], vr[it][1]};
        *(u32x2*)(dst + 8) = (u32x2){vr[it][2], vr[it][3]};
      }
    }
  }
  bf16x8 qf0[2], qf1[2];
#pragma unroll
  for (int rep = 0; rep < 2; ++rep) {
    const u32x2* src = (const u32x2*)(qb + (size_t)(q0 + 32 * (w + 4 * rep) + r) * D_ + h * DH_);
    u32x2 a = src[2 * hh], b = src[2 * hh + 1], c = {0, 0};
    if (hh == 0) c = src[4];
    u32x4 f0 = {a[0], a[1], b[0], b[1]}, f1 = {c[0], c[1], 0u, 0u};
    qf0[rep] = __builtin_bit_cast(bf16x8, f0); qf1[rep] = __builtin_bit_cast(bf16x8, f1);
  }
  __syncthreads();
  const float NEG = -__builtin_inff();
  f32x16 zero;
#pragma unroll
  for (int i = 0; i < 16; ++i) zero[i] = 0.f;
  const float C2 = 0.22360679774997896f * 1.4426950408889634f;
  const bool dval = r <= DH_;
  const char* vrow = Vs + (dval ? r : 0) * VS_STRIDE;
#pragma unroll
  for (int rep = 0; rep < 2; ++rep) {
    const int wq = w + 4 * rep, qw = q0 + 32 * wq;
    const int t_lo = (qw >= WIN_) ? 0 : (WIN_ - qw) / 32;
    f32x16 sc[9];
    float mx = NEG;
#pragma unroll
    for (int t = 0; t < 9; ++t) {
      if (t >= t_lo) {
        const int row = 32 * (wq + t) + r, sw = (row >> 2) & 3;
        bf16x8 k0 = *(const bf16x8*)(Ks + row * 64 + ((hh ^ sw) << 4));
        bf16x8 k1 = *(const bf16x8*)(Ks + row * 64 + (((2 + hh) ^ sw) << 4));
        f32x16 z = MFMA32(k0, qf0[rep], zero);
        z = MFMA32(k1, qf1[rep], z);
        if (t == 0) {
#pragma unroll
          for (int i = 0; i < 16; ++i) if ((i & 3) + 8 * (i >> 2) + 4 * hh <= r) z[i] = NEG;
        }
        if (t == 8) {
#pragma unroll
          for (int i = 0; i < 16; ++i) if ((i & 3) + 8 * (i >> 2) + 4 * hh > r) z[i] = NEG;
        }
        sc[t] = z;
#pragma unroll
        for (int i = 0; i < 16; i += 2) mx = max3f(mx, z[i], z[i + 1]);
      }
    }
    mx = fmaxf(mx, __shfl_xor(mx, 32));
    const float mo = -mx * C2;
    f32x16 oacc = zero;
#pragma unroll
    for (int t = 0; t < 9; ++t) {
      if (t >= t_lo) {
        float pv[16];
#pragma unroll
        for (int i = 0; i < 16; ++i) pv[i] = __builtin_amdgcn_exp2f(fmaf(sc[t][i], C2, mo));
#pragma unroll
        for (int s = 0; s < 2; ++s) {
          u32x4 pp = {pk2(pv[8 * s], pv[8 * s + 1]), pk2(pv[8 * s + 2], pv[8 * s + 3]), pk2(pv[8 * s + 4], pv[8 * s + 5]), pk2(pv[8 * s + 6], pv[8 * s + 7])};
          const int kidx = 32 * (wq + t) + 16 * s + 4 * hh;
          u32x2 v0 = *(const u32x2*)(vrow + kidx * 2);
          u32x2 v1 = *(const u32x2*)(vrow + kidx * 2 + 16);
          u32x4 vv = {v0[0], v0[1], v1[0], v1[1]};
          if (!dval) vv = (u32x4){0u, 0u, 0u, 0u};
          oacc = MFMA32(__builtin_bit_cast(bf16x8, vv), __builtin_bit_cast(bf16x8, pp), oacc);
        }
      }
    }
    const float inv = 1.f / __shfl(oacc[8], r + 32);
    u16* orow = ob + (size_t)(qw + r) * D_ + h * DH_ + 4 * hh;
#pragma unroll
    for (int qd = 0; qd < 3; ++qd) {
      if (qd < 2 || hh == 0) {
        u32x2 o = {pk2(oacc[4 * qd] * inv, oacc[4 * qd + 1] * inv), pk2(oacc[4 * qd + 2] * inv, oacc[4 * qd + 3] * inv)};
        *(u32x2*)(orow + 8 * qd) = o;
      }
    }
  }
}

typedef Gemm<64, 64, 2, 2, 4, 10, true, false> G3;

__global__ __launch_bounds__(256, 2) void k3_out(const u16* __restrict__ ob, const u16* __restrict__ wo, const u16* __restrict__ gb,
                                                 const u16* __restrict__ x, u16* __restrict__ out) {
  __shared__ __attribute__((aligned(16))) char lds[G3::LDS_BYTES];
  const int tid = threadIdx.x, lane = tid & 63, w = tid >> 6, r = lane & 31, hh = lane >> 5, wm = w >> 1, wn = w & 1;
  const int xcd = blockIdx.x & 7, idx = blockIdx.x >> 3;
  const int mt = xcd * 4 + idx / 10, nt = idx % 10;
  u32x4 gv[2], xv[2];
#pragma unroll
  for (int i = 0; i < 2; ++i) {
    const size_t off = (size_t)(mt * 64 + wm * 32 + i * 16 + (lane >> 2)) * D_ + nt * 64 + wn * 32 + (lane & 3) * 8;
    gv[i] = *(const u32x4*)(gb + off);
    xv[i] = *(const u32x4*)(x + off);
  }
  f32x16 acc[1][1]; float d0, d1, d2[1];
  G3::run(lds, ob + (size_t)mt * 64 * D_, D_, wo + nt * 64, D_, acc, d0, d1, d2);
  char* ep = lds + w * (32 * EP2_RS);
#pragma unroll
  for (int qd = 0; qd < 4; ++qd)
    *(u32x2*)(ep + r * EP2_RS + (8 * qd + 4 * hh) * 2) = (u32x2){pk2(acc[0][0][4 * qd], acc[0][0][4 * qd + 1]), pk2(acc[0][0][4 * qd + 2], acc[0][0][4 * qd + 3])};
  wave_lds_sync();
#pragma unroll
  for (int i = 0; i < 2; ++i) {
    const int row = i * 16 + (lane >> 2), ch = lane & 3;
    u32x4 sv = *(const u32x4*)(ep + row * EP2_RS + ch * 16);
    float o[8];
#pragma unroll
    for (int j = 0; j < 8; ++j) {
      const float g = bf2f((u16)(gv[i][j >> 1] >> (16 * (j & 1))));
      const float xx = bf2f((u16)(xv[i][j >> 1] >> (16 * (j & 1))));
      const float sf = bf2f((u16)(sv[j >> 1] >> (16 * (j & 1))));
      const float sg = rbf(g / (1.f + __expf(-g)));
      o[j] = xx + rbf(sf * sg);
    }
    u32x4 ov = {pk2(o[0], o[1]), pk2(o[2], o[3]), pk2(o[4], o[5]), pk2(o[6], o[7])};
    *(u32x4*)(out + (size_t)(mt * 64 + wm * 32 + row) * D_ + nt * 64 + wn * 32 + ch * 8) = ov;
  }
}

extern "C" void kernel_launch(void* const* d_in, const int* in_sizes, int n_in,
                              void* d_out, int out_size, void* d_ws, size_t ws_size,
                              hipStream_t stream) {
  const u16* x = (const u16*)d_in[0];
  const u16* wqkv = (const u16*)d_in[1];
  const u16* wo = (const u16*)d_in[2];
  const u16* w1 = (const u16*)d_in[4];
  const u16* w2 = (const u16*)d_in[5];
  u16* out = (u16*)d_out;
  char* ws = (char*)d_ws;
  const size_t SD = (size_t)S_ * D_ * 2;
  u16* qb = (u16*)(ws);
  u16* kb = (u16*)(ws + SD);
  u16* vT = (u16*)(ws + 2 * SD);
  u16* ob = (u16*)(ws + 3 * SD);
  u16* gb = (u16*)(ws + 4 * SD);
  u16* hb = (u16*)(ws + 5 * SD);
  k1_qkv_h<<<8 * 27, 512, 0, stream>>>(x, wqkv, w1, qb, kb, vT, hb);
  k2_attn_w2<<<G2_BLOCKS + ATT_BLOCKS, 256, 0, stream>>>(qb, kb, vT, hb, w2, ob, gb);
  k3_out<<<(S_ / 64) * (D_ / 64), 256, 0, stream>>>(ob, wo, gb, x, out);
}
```

```cpp
#include <hip/hip_runtime.h>
#include <hip/hip_bf16.h>
#include <stdint.h>

typedef short bf16x8 __attribute__((ext_vector_type(8)));
typedef short s16x4 __attribute__((ext_vector_type(4)));
typedef __bf16 bf16x2_t __attribute__((ext_vector_type(2)));
typedef __bf16 bf16x4_t __attribute__((ext_vector_type(4)));
typedef float f32x16 __attribute__((ext_vector_type(16)));
typedef float f32x4 __attribute__((ext_vector_type(4)));
typedef unsigned u32x4 __attribute__((ext_vector_type(4)));
typedef unsigned u32x2 __attribute__((ext_vector_type(2)));
typedef unsigned short u16;

#define DI __device__ __forceinline__
#define MFMA32(a, b, c) __builtin_amdgcn_mfma_f32_32x32x16_bf16((a), (b), (c), 0, 0, 0)


constexpr int S_ = 2048, D_ = 640, FF_ = 1536, NH_ = 32, DH_ = 20, WIN_ = 256;

template <int N> DI void wait_vm_t() { asm volatile("s_waitcnt vmcnt(%0)" ::"n"(N) : "memory"); }
DI void wait_vm(int n) {
  switch (n) {
    case 0: wait_vm_t<0>(); break;   case 2: wait_vm_t<2>(); break;   case 3: wait_vm_t<3>(); break;
    case 4: wait_vm_t<4>(); break;   case 6: wait_vm_t<6>(); break;   case 8: wait_vm_t<8>(); break;
    case 9: wait_vm_t<9>(); break;   case 12: wait_vm_t<12>(); break; case 16: wait_vm_t<16>(); break;
    case 18: wait_vm_t<18>(); break; case 24: wait_vm_t<24>(); break; case 30: wait_vm_t<30>(); break;
    default: wait_vm_t<0>(); break;
  }
}
template <int N> DI void wait_lgkm() { asm volatile("s_waitcnt lgkmcnt(%0)" ::"n"(N) : "memory"); }
DI void bar() {
  asm volatile("s_waitcnt lgkmcnt(0)" ::: "memory");
  __builtin_amdgcn_s_barrier();
  asm volatile("" ::: "memory");
}
DI void wave_lds_sync() { asm volatile("s_waitcnt lgkmcnt(0)" ::: "memory"); __builtin_amdgcn_wave_barrier(); }
DI u16 f2bf(float f) { return __builtin_bit_cast(u16, (__bf16)f); }
DI float bf2f(u16 v) { return __builtin_bit_cast(float, (unsigned)v << 16); }
DI float rbf(float f) { return bf2f(f2bf(f)); }
typedef float f32x2 __attribute__((ext_vector_type(2)));
DI unsigned pk2(float a, float b) { f32x2 v = {a, b}; return __builtin_bit_cast(unsigned, __builtin_convertvector(v, bf16x2_t)); }
DI float max2f(float a, float b) { return __builtin_amdgcn_fmed3f(a, b, __builtin_inff()); }
DI float dot2acc(unsigned a, unsigned b, float c) {
  return __builtin_amdgcn_fdot2_f32_bf16(__builtin_bit_cast(bf16x2_t, a), __builtin_bit_cast(bf16x2_t, b), c, false);
}
DI void dot2v(float& acc, unsigned a, unsigned b) { asm volatile("v_dot2c_f32_bf16 %0, %1, %2" : "+v"(acc) : "v"(a), "v"(b)); }
DI s16x4 tr_read(const char* p) {
  bf16x4_t v = __builtin_amdgcn_ds_read_tr16_b64_v4bf16((__attribute__((address_space(3))) bf16x4_t*)(p));
  return __builtin_bit_cast(s16x4, v);
}
DI void glds16(const void* g, char* l) {
  __builtin_amdgcn_global_load_lds((const __attribute__((address_space(1))) void*)g,
                                   (__attribute__((address_space(3))) void*)l, 16, 0, 0);
}

template <int BM, int BN, int WM, int WN, int NS, int NK, bool SWAP, bool STATS>
struct Gemm {
  static constexpr int NTH = WM * WN * 64;
  static constexpr int MT = BM / WM / 32, NT = BN / WN / 32;
  static constexpr int A_BYTES = BM * 128, B_BYTES = 64 * BN * 2, STAGE_BYTES = A_BYTES + B_BYTES;
  static constexpr int A_GLDS = BM * 8 / NTH, B_CPR = BN / 8, B_GLDS = 64 * B_CPR / NTH;
  static constexpr int NG = A_GLDS + B_GLDS;
  static constexpr int LDS_BYTES = NS * STAGE_BYTES;

  DI static int swzB(int k) { return (B_CPR == 16) ? ((k & 3) << 2) : (((k >> 1) & 1) << 2); }

  DI static void init_ptrs(const u16* __restrict__ A, int lda, const u16* __restrict__ B, int ldb, int tid,
                           const u16* (&pa)[A_GLDS], const u16* (&pb)[B_GLDS]) {
#pragma unroll
    for (int i = 0; i < A_GLDS; ++i) {
      const int ci = i * NTH + tid, row = ci >> 3, c = (ci & 7) ^ ((row >> 1) & 7);
      pa[i] = A + (size_t)row * lda + c * 8;
    }
#pragma unroll
    for (int i = 0; i < B_GLDS; ++i) {
      const int ci = i * NTH + tid, k = ci / B_CPR, c = (ci % B_CPR) ^ swzB(k);
      pb[i] = B + (size_t)k * ldb + c * 8;
    }
  }
  DI static void stage(char* buf, const u16* (&pa)[A_GLDS], const u16* (&pb)[B_GLDS], int ldb, int wbase) {
#pragma unroll
    for (int i = 0; i < A_GLDS; ++i) {
      glds16(pa[i], buf + i * (NTH * 16) + wbase);
      pa[i] += 64; asm volatile("" : "+v"(pa[i]));
    }
#pragma unroll
    for (int i = 0; i < B_GLDS; ++i) {
      glds16(pb[i], buf + A_BYTES + i * (NTH * 16) + wbase);
      pb[i] += (size_t)64 * ldb; asm volatile("" : "+v"(pb[i]));
    }
  }

  DI static void run(char* lds, const u16* __restrict__ A, int lda, const u16* __restrict__ B, int ldb,
                     f32x16 (&acc)[MT][NT], float& rsum, float& rsq, float (&csum)[NT]) {
    const int tid = threadIdx.x, lane = tid & 63;
    const int w = __builtin_amdgcn_readfirstlane(tid >> 6), wm = w / WN, wn = w % WN;
    const int r = lane & 31, hh = lane >> 5;
    const int blk = (lane >> 4) & 1, q = (lane & 15) >> 2, p = lane & 3;
#pragma unroll
    for (int mi = 0; mi < MT; ++mi)
#pragma unroll
      for (int ni = 0; ni < NT; ++ni)
#pragma unroll
        for (int i = 0; i < 16; ++i) acc[mi][ni][i] = 0.f;
    rsum = 0.f; rsq = 0.f;
#pragma unroll
    for (int ni = 0; ni < NT; ++ni) csum[ni] = 0.f;
    const unsigned ONES = 0x3f803f80u;
    float rs4[4] = {0.f, 0.f, 0.f, 0.f}, rq4[4] = {0.f, 0.f, 0.f, 0.f};
    unsigned ones = ONES; asm volatile("" : "+v"(ones));
    unsigned a_off[MT][4], b_off[NT];
#pragma unroll
    for (int mi = 0; mi < MT; ++mi) {
      const int row = (wm * MT + mi) * 32 + r;
#pragma unroll
      for (int s = 0; s < 4; ++s) a_off[mi][s] = row * 128 + (((2 * s + hh) ^ ((row >> 1) & 7)) << 4);
    }
#pragma unroll
    for (int ni = 0; ni < NT; ++ni) {
      const int cc = ((wn * NT + ni) * 32 + blk * 16 + 4 * p) >> 3, k1 = 8 * hh + q;
      b_off[ni] = A_BYTES + k1 * (BN * 2) + ((cc ^ swzB(k1)) << 4) + (p & 1) * 8;
    }

    const u16* pa[A_GLDS]; const u16* pb[B_GLDS];
    init_ptrs(A, lda, B, ldb, tid, pa, pb);
    const int wbase = w * 1024;
#pragma unroll
    for (int kt = 0; kt < NS - 1; ++kt) stage(lds + kt * STAGE_BYTES, pa, pb, ldb, wbase);
#pragma unroll
    for (int kt = 0; kt < NK; ++kt) {
      {
        const int last = (kt + NS - 2 < NK - 1) ? (kt + NS - 2) : (NK - 1);
        wait_vm((last - kt) * NG);
      }
      bar();
      if (kt + NS - 1 < NK) stage(lds + ((kt + NS - 1) % NS) * STAGE_BYTES, pa, pb, ldb, wbase);
      {
        const char* sb = lds + (kt % NS) * STAGE_BYTES;
#pragma unroll
        for (int s = 0; s < 4; ++s) {
          bf16x8 af[MT], bfr[NT];
#pragma unroll
          for (int mi = 0; mi < MT; ++mi) af[mi] = *(const bf16x8*)(sb + a_off[mi][s]);
#pragma unroll
          for (int ni = 0; ni < NT; ++ni) {
            s16x4 lo = tr_read(sb + b_off[ni] + s * 16 * BN * 2);
            s16x4 hi = tr_read(sb + b_off[ni] + s * 16 * BN * 2 + 4 * BN * 2);
            bfr[ni] = __builtin_shufflevector(lo, hi, 0, 1, 2, 3, 4, 5, 6, 7);
          }
#pragma unroll
          for (int mi = 0; mi < MT; ++mi)
#pragma unroll
            for (int ni = 0; ni < NT; ++ni)
              acc[mi][ni] = SWAP ? MFMA32(bfr[ni], af[mi], acc[mi][ni]) : MFMA32(af[mi], bfr[ni], acc[mi][ni]);
          if (STATS) {
            u32x4 u = __builtin_bit_cast(u32x4, af[0]);
#pragma unroll
            for (int mi = 1; mi < MT; ++mi) if (wn == mi) u = __builtin_bit_cast(u32x4, af[mi]);
#pragma unroll
            for (int j = 0; j < 4; ++j) { dot2v(rs4[j], u[j], ones); dot2v(rq4[j], u[j], u[j]); }
          }
        }
        if (STATS) {
#pragma unroll
          for (int ni = 0; ni < NT; ++ni) {
            s16x4 lo = tr_read(sb + b_off[ni] + wm * 16 * BN * 2);
            s16x4 hi = tr_read(sb + b_off[ni] + wm * 16 * BN * 2 + 4 * BN * 2);
            u32x2 u0 = __builtin_bit_cast(u32x2, lo), u1 = __builtin_bit_cast(u32x2, hi);
            dot2v(csum[ni], u0[0], ones); dot2v(csum[ni], u0[1], ones); dot2v(csum[ni], u1[0], ones); dot2v(csum[ni], u1[1], ones);
          }
        }
      }
    }
    rsum = (rs4[0] + rs4[1]) + (rs4[2] + rs4[3]); rsq = (rq4[0] + rq4[1]) + (rq4[2] + rq4[3]);
    bar();
  }
};

typedef Gemm<256, 128, 4, 2, 3, 10, true, true> G1s;
typedef Gemm<256, 128, 4, 2, 3, 10, false, true> G1n;
constexpr int EP_RS = 144;
constexpr int EP_WAVE = 64 * EP_RS;

__global__ __launch_bounds__(512, 2) void k1_qkv_h(const u16* __restrict__ x, const u16* __restrict__ wqkv, const u16* __restrict__ w1,
                                                   u16* __restrict__ qb, u16* __restrict__ kb, u16* __restrict__ vT, u16* __restrict__ hb) {
  __shared__ __attribute__((aligned(16))) char lds[G1s::LDS_BYTES];
  int mt, nt;
  { const int xcd = blockIdx.x & 7, idx = blockIdx.x >> 3;
    const int j = (xcd & 3) * 27 + idx; mt = (xcd >> 2) * 4 + (j & 3); nt = j >> 2; }
  const int tid = threadIdx.x, lane = tid & 63, w = tid >> 6, wm = w >> 1, wn = w & 1, r = lane & 31, hh = lane >> 5;
  const u16* A = x + (size_t)mt * 256 * D_;
  const u16* B; int ldb;
  if (nt < 15) { B = wqkv + nt * 128; ldb = 3 * D_; } else { B = w1 + (nt - 15) * 128; ldb = FF_; }
  f32x16 acc[2][2]; float rsum, rsq, csum[2];
  const bool isV = (nt >= 10 && nt < 15);
  if (isV) G1n::run(lds, A, D_, B, ldb, acc, rsum, rsq, csum);
  else     G1s::run(lds, A, D_, B, ldb, acc, rsum, rsq, csum);
  float* st = (float*)(lds + 8 * EP_WAVE);
  {
    float s1 = rsum + __shfl_xor(rsum, 32), s2 = rsq + __shfl_xor(rsq, 32);
    float m = s1 * (1.f / D_), v = fmaxf(s2 * (1.f / D_) - m * m, 0.f);
    float c0 = csum[0] + __shfl_xor(csum[0], 32), c1 = csum[1] + __shfl_xor(csum[1], 32);
    if (hh == 0) {
      const int row = (wm * 2 + wn) * 32 + r;
      st[row] = m; st[256 + row] = rsqrtf(v + 1e-5f);
      st[512 + wm * 128 + wn * 64 + r] = c0; st[512 + wm * 128 + wn * 64 + 32 + r] = c1;
    }
  }
  bar();
  char* ep = lds + w * EP_WAVE;
  const int tok0 = mt * 256 + wm * 64, n0 = wn * 64;
  if (!isV) {
    u16* dst; int ldd, cbase; bool relu = false;
    if (nt < 5) { dst = qb; ldd = D_; cbase = nt * 128; }
    else if (nt < 10) { dst = kb; ldd = D_; cbase = (nt - 5) * 128; }
    else { dst = hb; ldd = FF_; cbase = (nt - 15) * 128; relu = true; }
    float mean[2], rstd[2];
#pragma unroll
    for (int mi = 0; mi < 2; ++mi) { mean[mi] = st[wm * 64 + mi * 32 + r]; rstd[mi] = st[256 + wm * 64 + mi * 32 + r]; }
#pragma unroll
    for (int ni = 0; ni < 2; ++ni) {
#pragma unroll
      for (int qd = 0; qd < 4; ++qd) {
        const float* cp = st + 512 + n0 + ni * 32 + 8 * qd + 4 * hh;
        f32x4 cs = *(const f32x4*)(cp) + *(const f32x4*)(cp + 128) + *(const f32x4*)(cp + 256) + *(const f32x4*)(cp + 384);
#pragma unroll
        for (int mi = 0; mi < 2; ++mi) {
          float v[4];
#pragma unroll
          for (int j = 0; j < 4; ++j) {
            float t = rstd[mi] * (acc[mi][ni][4 * qd + j] - mean[mi] * cs[j]);
            v[j] = relu ? fmaxf(t, 0.f) : t;
          }
          *(u32x2*)(ep + (mi * 32 + r) * EP_RS + (ni * 32 + 8 * qd + 4 * hh) * 2) = (u32x2){pk2(v[0], v[1]), pk2(v[2], v[3])};
        }
      }
    }
    wave_lds_sync();
#pragma unroll
    for (int i = 0; i < 8; ++i) {
      const int row = i * 8 + (lane >> 3), ch = lane & 7;
      u32x4 v = *(const u32x4*)(ep + row * EP_RS + ch * 16);
      *(u32x4*)(dst + (size_t)(tok0 + row) * ldd + cbase + n0 + ch * 8) = v;
    }
  } else {
    const int cbase = (nt - 10) * 128;
    float cs[2];
#pragma unroll
    for (int ni = 0; ni < 2; ++ni) {
      const float* cp = st + 512 + n0 + ni * 32 + r;
      cs[ni] = cp[0] + cp[128] + cp[256] + cp[384];
    }
#pragma unroll
    for (int mi = 0; mi < 2; ++mi) {
#pragma unroll
      for (int qd = 0; qd < 4; ++qd) {
        f32x4 mn = *(const f32x4*)(st + wm * 64 + mi * 32 + 8 * qd + 4 * hh);
        f32x4 rs = *(const f32x4*)(st + 256 + wm * 64 + mi * 32 + 8 * qd + 4 * hh);
#pragma unroll
        for (int ni = 0; ni < 2; ++ni) {
          float v[4];
#pragma unroll
          for (int j = 0; j < 4; ++j) v[j] = rs[j] * (acc[mi][ni][4 * qd + j] - mn[j] * cs[ni]);
          *(u32x2*)(ep + (ni * 32 + r) * EP_RS + (mi * 32 + 8 * qd + 4 * hh) * 2) = (u32x2){pk2(v[0], v[1]), pk2(v[2], v[3])};
        }
      }
    }
    wave_lds_sync();
#pragma unroll
    for (int i = 0; i < 8; ++i) {
      const int row = i * 8 + (lane >> 3), ch = lane & 7;
      u32x4 v = *(const u32x4*)(ep + row * EP_RS + ch * 16);
      *(u32x4*)(vT + (size_t)(cbase + n0 + row) * S_ + tok0 + ch * 8) = v;
    }
  }
}

typedef Gemm<128, 64, 2, 2, 3, 24, true, false> G2;
constexpr int G2_BLOCKS = (S_ / 128) * (D_ / 64);
constexpr int ATT_BLOCKS = NH_ * (S_ / 256);
constexpr int KS_BYTES = 512 * 64, VS_STRIDE = 1032;
constexpr int EP2_RS = 80;
static_assert(KS_BYTES + (DH_ + 1) * VS_STRIDE <= G2::LDS_BYTES, "lds");

__global__ __launch_bounds__(256, 2) void k2_attn_w2(const u16* __restrict__ qb, const u16* __restrict__ kb, const u16* __restrict__ vT,
                                                     const u16* __restrict__ hb, const u16* __restrict__ w2,
                                                     u16* __restrict__ ob, u16* __restrict__ gb) {
  __shared__ __attribute__((aligned(16))) char lds[G2::LDS_BYTES];
  const int tid = threadIdx.x, lane = tid & 63, w = __builtin_amdgcn_readfirstlane(tid >> 6), r = lane & 31, hh = lane >> 5;
  const int bid = blockIdx.x;
  if (bid < G2_BLOCKS) {
    const int xcd = bid & 7, idx = bid >> 3;
    const int mt = xcd * 2 + idx / 10, nt = idx % 10;
    f32x16 acc[2][1]; float d0, d1, d2[1];
    G2::run(lds, hb + (size_t)mt * 128 * FF_, FF_, w2 + nt * 64, D_, acc, d0, d1, d2);
    const int wm = w >> 1, wn = w & 1;
    char* ep = lds + w * (64 * EP2_RS);
#pragma unroll
    for (int mi = 0; mi < 2; ++mi)
#pragma unroll
      for (int qd = 0; qd < 4; ++qd)
        *(u32x2*)(ep + (mi * 32 + r) * EP2_RS + (8 * qd + 4 * hh) * 2) = (u32x2){pk2(acc[mi][0][4 * qd], acc[mi][0][4 * qd + 1]), pk2(acc[mi][0][4 * qd + 2], acc[mi][0][4 * qd + 3])};
    wave_lds_sync();
#pragma unroll
    for (int i = 0; i < 4; ++i) {
      const int row = i * 16 + (lane >> 2), ch = lane & 3;
      u32x4 v = *(const u32x4*)(ep + row * EP2_RS + ch * 16);
      *(u32x4*)(gb + (size_t)(mt * 128 + wm * 64 + row) * D_ + nt * 64 + wn * 32 + ch * 8) = v;
    }
    return;
  }
  const int ab = bid - G2_BLOCKS;
  const int h = ab >> 3, q0 = (ab & 7) * 256;
  const int kstart = q0 - WIN_;
  char* Ks = lds;
  char* Vs = lds + KS_BYTES;
  {
    u32x2 kr[2][5];
#pragma unroll
    for (int it = 0; it < 2; ++it) {
      const int key = kstart + tid + it * 256;
#pragma unroll
      for (int j = 0; j < 5; ++j) kr[it][j] = (u32x2){0u, 0u};
      if (key >= 0) {
        const u32x2* src = (const u32x2*)(kb + (size_t)key * D_ + h * DH_);
#pragma unroll
        for (int j = 0; j < 5; ++j) kr[it][j] = src[j];
      }
    }
    u32x4 vr[6];
#pragma unroll
    for (int it = 0; it < 6; ++it) {
      const int idx = tid + it * 256, d = idx >> 6, c = idx & 63, key0 = kstart + c * 8;
      vr[it] = (u32x4){0u, 0u, 0u, 0u};
      if (d == DH_) vr[it] = (u32x4){0x3f803f80u, 0x3f803f80u, 0x3f803f80u, 0x3f803f80u};
      else if (d < DH_ && key0 >= 0) vr[it] = *(const u32x4*)(vT + (size_t)(h * DH_ + d) * S_ + key0);
    }
#pragma unroll
    for (int it = 0; it < 2; ++it) {
      const int ki = tid + it * 256, sw = (ki >> 2) & 3;
      char* rowp = Ks + ki * 64;
      *(u32x4*)(rowp + ((0 ^ sw) << 4)) = (u32x4){kr[it][0][0], kr[it][0][1], kr[it][1][0], kr[it][1][1]};
      *(u32x4*)(rowp + ((1 ^ sw) << 4)) = (u32x4){kr[it][2][0], kr[it][2][1], kr[it][3][0], kr[it][3][1]};
      *(u32x4*)(rowp + ((2 ^ sw) << 4)) = (u32x4){kr[it][4][0], kr[it][4][1], 0u, 0u};
      *(u32x4*)(rowp + ((3 ^ sw) << 4)) = (u32x4){0u, 0u, 0u, 0u};
    }
#pragma unroll
    for (int it = 0; it < 6; ++it) {
      const int idx = tid + it * 256, d = idx >> 6, c = idx & 63;
      if (d <= DH_) {
        char* dst = Vs + d * VS_STRIDE + c * 16;
        *(u32x2*)(dst) = (u32x2){vr[it][0], vr[it][1]};
        *(u32x2*)(dst + 8) = (u32x2){vr[it][2], vr[it][3]};
      }
    }
  }
  bf16x8 qf0[2], qf1[2];
#pragma unroll
  for (int rep = 0; rep < 2; ++rep) {
    const u32x2* src = (const u32x2*)(qb + (size_t)(q0 + 32 * (w + 4 * rep) + r) * D_ + h * DH_);
    u32x2 a = src[2 * hh], b = src[2 * hh + 1], c = {0, 0};
    if (hh == 0) c = src[4];
    u32x4 f0 = {a[0], a[1], b[0], b[1]}, f1 = {c[0], c[1], 0u, 0u};
    qf0[rep] = __builtin_bit_cast(bf16x8, f0); qf1[rep] = __builtin_bit_cast(bf16x8, f1);
  }
  __syncthreads();
  const float NEG = -__builtin_inff();
  f32x16 zero;
#pragma unroll
  for (int i = 0; i < 16; ++i) zero[i] = 0.f;
  const float C2 = 0.22360679774997896f * 1.4426950408889634f;
  const bool dval = r <= DH_;
  const char* vrow = Vs + (dval ? r : 0) * VS_STRIDE;
#pragma unroll
  for (int rep = 0; rep < 2; ++rep) {
    const int wq = w + 4 * rep, qw = q0 + 32 * wq;
    const int t_lo = (qw >= WIN_) ? 0 : (WIN_ - qw) / 32;
    f32x16 sc[9];
    float mx = NEG, mx2 = NEG;
#pragma unroll
    for (int t = 0; t < 9; ++t) {
      if (t >= t_lo) {
        const int row = 32 * (wq + t) + r, sw = (row >> 2) & 3;
        bf16x8 k0 = *(const bf16x8*)(Ks + row * 64 + ((hh ^ sw) << 4));
        bf16x8 k1 = *(const bf16x8*)(Ks + row * 64 + (((2 + hh) ^ sw) << 4));
        f32x16 z = MFMA32(k0, qf0[rep], zero);
        z = MFMA32(k1, qf1[rep], z);
        if (t == 0) {
#pragma unroll
          for (int i = 0; i < 16; ++i) if ((i & 3) + 8 * (i >> 2) + 4 * hh <= r) z[i] = NEG;
        }
        if (t == 8) {
#pragma unroll
          for (int i = 0; i < 16; ++i) if ((i & 3) + 8 * (i >> 2) + 4 * hh > r) z[i] = NEG;
        }
        sc[t] = z;
#pragma unroll
        for (int i = 0; i < 16; i += 2) { mx = max2f(mx, z[i]); mx2 = max2f(mx2, z[i + 1]); }
      }
    }
    mx = fmaxf(mx, mx2);
    mx = fmaxf(mx, __shfl_xor(mx, 32));
    const float mo = -mx * C2;
    f32x16 oacc = zero;
#pragma unroll
    for (int t = 0; t < 9; ++t) {
      if (t >= t_lo) {
        float pv[16];
#pragma unroll
        for (int i = 0; i < 16; ++i) pv[i] = __builtin_amdgcn_exp2f(fmaf(sc[t][i], C2, mo));
#pragma unroll
        for (int s = 0; s < 2; ++s) {
          u32x4 pp = {pk2(pv[8 * s], pv[8 * s + 1]), pk2(pv[8 * s + 2], pv[8 * s + 3]), pk2(pv[8 * s + 4], pv[8 * s + 5]), pk2(pv[8 * s + 6], pv[8 * s + 7])};
          const int kidx = 32 * (wq + t) + 16 * s + 4 * hh;
          u32x2 v0 = *(const u32x2*)(vrow + kidx * 2);
          u32x2 v1 = *(const u32x2*)(vrow + kidx * 2 + 16);
          u32x4 vv = {v0[0], v0[1], v1[0], v1[1]};
          if (!dval) vv = (u32x4){0u, 0u, 0u, 0u};
          oacc = MFMA32(__builtin_bit_cast(bf16x8, vv), __builtin_bit_cast(bf16x8, pp), oacc);
        }
      }
    }
    const float inv = 1.f / __shfl(oacc[8], r + 32);
    u16* orow = ob + (size_t)(qw + r) * D_ + h * DH_ + 4 * hh;
#pragma unroll
    for (int qd = 0; qd < 3; ++qd) {
      if (qd < 2 || hh == 0) {
        u32x2 o = {pk2(oacc[4 * qd] * inv, oacc[4 * qd + 1] * inv), pk2(oacc[4 * qd + 2] * inv, oacc[4 * qd + 3] * inv)};
        *(u32x2*)(orow + 8 * qd) = o;
      }
    }
  }
}

typedef Gemm<64, 64, 2, 2, 4, 10, true, false> G3;

__global__ __launch_bounds__(256, 2) void k3_out(const u16* __restrict__ ob, const u16* __restrict__ wo, const u16* __restrict__ gb,
                                                 const u16* __restrict__ x, u16* __restrict__ out) {
  __shared__ __attribute__((aligned(16))) char lds[G3::LDS_BYTES];
  const int tid = threadIdx.x, lane = tid & 63, w = tid >> 6, r = lane & 31, hh = lane >> 5, wm = w >> 1, wn = w & 1;
  const int xcd = blockIdx.x & 7, idx = blockIdx.x >> 3;
  const int mt = xcd * 4 + idx / 10, nt = idx % 10;
  u32x4 gv[2], xv[2];
#pragma unroll
  for (int i = 0; i < 2; ++i) {
    const size_t off = (size_t)(mt * 64 + wm * 32 + i * 16 + (lane >> 2)) * D_ + nt * 64 + wn * 32 + (lane & 3) * 8;
    gv[i] = *(const u32x4*)(gb + off);
    xv[i] = *(const u32x4*)(x + off);
  }
  f32x16 acc[1][1]; float d0, d1, d2[1];
  G3::run(lds, ob + (size_t)mt * 64 * D_, D_, wo + nt * 64, D_, acc, d0, d1, d2);
  char* ep = lds + w * (32 * EP2_RS);
#pragma unroll
  for (int qd = 0; qd < 4; ++qd)
    *(u32x2*)(ep + r * EP2_RS + (8 * qd + 4 * hh) * 2) = (u32x2){pk2(acc[0][0][4 * qd], acc[0][0][4 * qd + 1]), pk2(acc[0][0][4 * qd + 2], acc[0][0][4 * qd + 3])};
  wave_lds_sync();
#pragma unroll
  for (int i = 0; i < 2; ++i) {
    const int row = i * 16 + (lane >> 2), ch = lane & 3;
    u32x4 sv = *(const u32x4*)(ep + row * EP2_RS + ch * 16);
    float o[8];
#pragma unroll
    for (int j = 0; j < 8; ++j) {
      const float g = bf2f((u16)(gv[i][j >> 1] >> (16 * (j & 1))));
      const float xx = bf2f((u16)(xv[i][j >> 1] >> (16 * (j & 1))));
      const float sf = bf2f((u16)(sv[j >> 1] >> (16 * (j & 1))));
      const float sg = rbf(g / (1.f + __expf(-g)));
      o[j] = xx + rbf(sf * sg);
    }
    u32x4 ov = {pk2(o[0], o[1]), pk2(o[2], o[3]), pk2(o[4], o[5]), pk2(o[6], o[7])};
    *(u32x4*)(out + (size_t)(mt * 64 + wm * 32 + row) * D_ + nt * 64 + wn * 32 + ch * 8) = ov;
  }
}

extern "C" void kernel_launch(void* const* d_in, const int* in_sizes, int n_in,
                              void* d_out, int out_size, void* d_ws, size_t ws_size,
                              hipStream_t stream) {
  const u16* x = (const u16*)d_in[0];
  const u16* wqkv = (const u16*)d_in[1];
  const u16* wo = (const u16*)d_in[2];
  const u16* w1 = (const u16*)d_in[4];
  const u16* w2 = (const u16*)d_in[5];
  u16* out = (u16*)d_out;
  char* ws = (char*)d_ws;
  const size_t SD = (size_t)S_ * D_ * 2;
  u16* qb = (u16*)(ws);
  u16* kb = (u16*)(ws + SD);
  u16* vT = (u16*)(ws + 2 * SD);
  u16* ob = (u16*)(ws + 3 * SD);
  u16* gb = (u16*)(ws + 4 * SD);
  u16* hb = (u16*)(ws + 5 * SD);
  k1_qkv_h<<<8 * 27, 512, 0, stream>>>(x, wqkv, w1, qb, kb, vT, hb);
  k2_attn_w2<<<G2_BLOCKS + ATT_BLOCKS, 256, 0, stream>>>(qb, kb, vT, hb, w2, ob, gb);
  k3_out<<<(S_ / 64) * (D_ / 64), 256, 0, stream>>>(ob, wo, gb, x, out);
}
```

```cpp
#include <hip/hip_runtime.h>
#include <hip/hip_bf16.h>
#include <stdint.h>

typedef short bf16x8 __attribute__((ext_vector_type(8)));
typedef short s16x4 __attribute__((ext_vector_type(4)));
typedef __bf16 bf16x2_t __attribute__((ext_vector_type(2)));
typedef __bf16 bf16x4_t __attribute__((ext_vector_type(4)));
typedef float f32x16 __attribute__((ext_vector_type(16)));
typedef float f32x4 __attribute__((ext_vector_type(4)));
typedef unsigned u32x4 __attribute__((ext_vector_type(4)));
typedef unsigned u32x2 __attribute__((ext_vector_type(2)));
typedef unsigned short u16;

#define DI __device__ __forceinline__
#define MFMA32(a, b, c) __builtin_amdgcn_mfma_f32_32x32x16_bf16((a), (b), (c), 0, 0, 0)


constexpr int S_ = 2048, D_ = 640, FF_ = 1536, NH_ = 32, DH_ = 20, WIN_ = 256;

template <int N> DI void wait_vm_t() { asm volatile("s_waitcnt vmcnt(%0)" ::"n"(N) : "memory"); }
DI void wait_vm(int n) {
  switch (n) {
    case 0: wait_vm_t<0>(); break;   case 2: wait_vm_t<2>(); break;   case 3: wait_vm_t<3>(); break;
    case 4: wait_vm_t<4>(); break;   case 6: wait_vm_t<6>(); break;   case 8: wait_vm_t<8>(); break;
    case 9: wait_vm_t<9>(); break;   case 12: wait_vm_t<12>(); break; case 16: wait_vm_t<16>(); break;
    case 18: wait_vm_t<18>(); break; case 24: wait_vm_t<24>(); break; case 30: wait_vm_t<30>(); break;
    default: wait_vm_t<0>(); break;
  }
}
template <int N> DI void wait_lgkm() { asm volatile("s_waitcnt lgkmcnt(%0)" ::"n"(N) : "memory"); }
DI void bar() {
  asm volatile("s_waitcnt lgkmcnt(0)" ::: "memory");
  __builtin_amdgcn_s_barrier();
  asm volatile("" ::: "memory");
}
DI void wave_lds_sync() { asm volatile("s_waitcnt lgkmcnt(0)" ::: "memory"); __builtin_amdgcn_wave_barrier(); }
DI u16 f2bf(float f) { return __builtin_bit_cast(u16, (__bf16)f); }
DI float bf2f(u16 v) { return __builtin_bit_cast(float, (unsigned)v << 16); }
DI float rbf(float f) { return bf2f(f2bf(f)); }
typedef float f32x2 __attribute__((ext_vector_type(2)));
DI unsigned pk2(float a, float b) { f32x2 v = {a, b}; return __builtin_bit_cast(unsigned, __builtin_convertvector(v, bf16x2_t)); }
DI float max2f(float a, float b) { return __builtin_amdgcn_fmed3f(a, b, __builtin_inff()); }
DI float dot2acc(unsigned a, unsigned b, float c) {
  return __builtin_amdgcn_fdot2_f32_bf16(__builtin_bit_cast(bf16x2_t, a), __builtin_bit_cast(bf16x2_t, b), c, false);
}
DI void dot2v(float& acc, unsigned a, unsigned b) { asm volatile("v_dot2c_f32_bf16 %0, %1, %2" : "+v"(acc) : "v"(a), "v"(b)); }
DI s16x4 tr_read(const char* p) {
  bf16x4_t v = __builtin_amdgcn_ds_read_tr16_b64_v4bf16((__attribute__((address_space(3))) bf16x4_t*)(p));
  return __builtin_bit_cast(s16x4, v);
}
DI void glds16(const void* g, char* l) {
  __builtin_amdgcn_global_load_lds((const __attribute__((address_space(1))) void*)g,
                                   (__attribute__((address_space(3))) void*)l, 16, 0, 0);
}

template <int BM, int BN, int WM, int WN, int NS, int NK, bool SWAP, bool STATS>
struct Gemm {
  static constexpr int NTH = WM * WN * 64;
  static constexpr int MT = BM / WM / 32, NT = BN / WN / 32;
  static constexpr int A_BYTES = BM * 128, B_BYTES = 64 * BN * 2, STAGE_BYTES = A_BYTES + B_BYTES;
  static constexpr int A_GLDS = BM * 8 / NTH, B_CPR = BN / 8, B_GLDS = 64 * B_CPR / NTH;
  static constexpr int NG = A_GLDS + B_GLDS;
  static constexpr int LDS_BYTES = NS * STAGE_BYTES;

  DI static int swzB(int k) { return (B_CPR == 16) ? ((k & 3) << 2) : (((k >> 1) & 1) << 2); }

  DI static void init_ptrs(const u16* __restrict__ A, int lda, const u16* __restrict__ B, int ldb, int tid,
                           const u16* (&pa)[A_GLDS], const u16* (&pb)[B_GLDS]) {
#pragma unroll
    for (int i = 0; i < A_GLDS; ++i) {
      const int ci = i * NTH + tid, row = ci >> 3, c = (ci & 7) ^ ((row >> 1) & 7);
      pa[i] = A + (size_t)row * lda + c * 8;
    }
#pragma unroll
    for (int i = 0; i < B_GLDS; ++i) {
      const int ci = i * NTH + tid, k = ci / B_CPR, c = (ci % B_CPR) ^ swzB(k);
      pb[i] = B + (size_t)k * ldb + c * 8;
    }
  }
  DI static void stage(char* buf, const u16* (&pa)[A_GLDS], const u16* (&pb)[B_GLDS], int ldb, int wbase) {
#pragma unroll
    for (int i = 0; i < A_GLDS; ++i) {
      glds16(pa[i], buf + i * (NTH * 16) + wbase);
      pa[i] += 64; asm volatile("" : "+v"(pa[i]));
    }
#pragma unroll
    for (int i = 0; i < B_GLDS; ++i) {
      glds16(pb[i], buf + A_BYTES + i * (NTH * 16) + wbase);
      pb[i] += (size_t)64 * ldb; asm volatile("" : "+v"(pb[i]));
    }
  }

  DI static void run(char* lds, const u16* __restrict__ A, int lda, const u16* __restrict__ B, int ldb,
                     f32x16 (&acc)[MT][NT], float& rsum, float& rsq, float (&csum)[NT]) {
    const int tid = threadIdx.x, lane = tid & 63;
    const int w = __builtin_amdgcn_readfirstlane(tid >> 6), wm = w / WN, wn = w % WN;
    const int r = lane & 31, hh = lane >> 5;
    const int blk = (lane >> 4) & 1, q = (lane & 15) >> 2, p = lane & 3;
#pragma unroll
    for (int mi = 0; mi < MT; ++mi)
#pragma unroll
      for (int ni = 0; ni < NT; ++ni)
#pragma unroll
        for (int i = 0; i < 16; ++i) acc[mi][ni][i] = 0.f;
    rsum = 0.f; rsq = 0.f;
#pragma unroll
    for (int ni = 0; ni < NT; ++ni) csum[ni] = 0.f;
    const unsigned ONES = 0x3f803f80u;
    float rs4[4] = {0.f, 0.f, 0.f, 0.f}, rq4[4] = {0.f, 0.f, 0.f, 0.f};
    unsigned ones = ONES; asm volatile("" : "+v"(ones));
    unsigned a_off[MT][4], b_off[NT];
#pragma unroll
    for (int mi = 0; mi < MT; ++mi) {
      const int row = (wm * MT + mi) * 32 + r;
#pragma unroll
      for (int s = 0; s < 4; ++s) a_off[mi][s] = row * 128 + (((2 * s + hh) ^ ((row >> 1) & 7)) << 4);
    }
#pragma unroll
    for (int ni = 0; ni < NT; ++ni) {
      const int cc = ((wn * NT + ni) * 32 + blk * 16 + 4 * p) >> 3, k1 = 8 * hh + q;
      b_off[ni] = A_BYTES + k1 * (BN * 2) + ((cc ^ swzB(k1)) << 4) + (p & 1) * 8;
    }

    const u16* pa[A_GLDS]; const u16* pb[B_GLDS];
    init_ptrs(A, lda, B, ldb, tid, pa, pb);
    const int wbase = w * 1024;
#pragma unroll
    for (int kt = 0; kt < NS - 1; ++kt) stage(lds + kt * STAGE_BYTES, pa, pb, ldb, wbase);
#pragma unroll
    for (int kt = 0; kt < NK; ++kt) {
      {
        const int last = (kt + NS - 2 < NK - 1) ? (kt + NS - 2) : (NK - 1);
        wait_vm((last - kt) * NG);
      }
      bar();
      if (kt + NS - 1 < NK) stage(lds + ((kt + NS - 1) % NS) * STAGE_BYTES, pa, pb, ldb, wbase);
      {
        const char* sb = lds + (kt % NS) * STAGE_BYTES;
#pragma unroll
        for (int s = 0; s < 4; ++s) {
          bf16x8 af[MT], bfr[NT];
#pragma unroll
          for (int mi = 0; mi < MT; ++mi) af[mi] = *(const bf16x8*)(sb + a_off[mi][s]);
#pragma unroll
          for (int ni = 0; ni < NT; ++ni) {
            s16x4 lo = tr_read(sb + b_off[ni] + s * 16 * BN * 2);
            s16x4 hi = tr_read(sb + b_off[ni] + s * 16 * BN * 2 + 4 * BN * 2);
            bfr[ni] = __builtin_shufflevector(lo, hi, 0, 1, 2, 3, 4, 5, 6, 7);
          }
#pragma unroll
          for (int mi = 0; mi < MT; ++mi)
#pragma unroll
            for (int ni = 0; ni < NT; ++ni)
              acc[mi][ni] = SWAP ? MFMA32(bfr[ni], af[mi], acc[mi][ni]) : MFMA32(af[mi], bfr[ni], acc[mi][ni]);
          if (STATS) {
            u32x4 u = __builtin_bit_cast(u32x4, af[0]);
#pragma unroll
            for (int mi = 1; mi < MT; ++mi) if (wn == mi) u = __builtin_bit_cast(u32x4, af[mi]);
#pragma unroll
            for (int j = 0; j < 4; ++j) { dot2v(rs4[j], u[j], ones); dot2v(rq4[j], u[j], u[j]); }
          }
        }
        if (STATS) {
#pragma unroll
          for (int ni = 0; ni < NT; ++ni) {
            s16x4 lo = tr_read(sb + b_off[ni] + wm * 16 * BN * 2);
            s16x4 hi = tr_read(sb + b_off[ni] + wm * 16 * BN * 2 + 4 * BN * 2);
            u32x2 u0 = __builtin_bit_cast(u32x2, lo), u1 = __builtin_bit_cast(u32x2, hi);
            dot2v(csum[ni], u0[0], ones); dot2v(csum[ni], u0[1], ones); dot2v(csum[ni], u1[0], ones); dot2v(csum[ni], u1[1], ones);
          }
        }
      }
    }
    rsum = (rs4[0] + rs4[1]) + (rs4[2] + rs4[3]); rsq = (rq4[0] + rq4[1]) + (rq4[2] + rq4[3]);
    bar();
  }
};

typedef Gemm<256, 128, 4, 2, 3, 10, true, true> G1s;
typedef Gemm<256, 128, 4, 2, 3, 10, false, true> G1n;
constexpr int EP_RS = 144;
constexpr int EP_WAVE = 64 * EP_RS;

__global__ __launch_bounds__(512, 2) void k1_qkv_h(const u16* __restrict__ x, const u16* __restrict__ wqkv, const u16* __restrict__ w1,
                                                   u16* __restrict__ qb, u16* __restrict__ kb, u16* __restrict__ vT, u16* __restrict__ hb,
                                                   const u16* __restrict__ w2, const u16* __restrict__ wo) {
  __shared__ __attribute__((aligned(16))) char lds[G1s::LDS_BYTES];
  if (blockIdx.x >= 216) {
    const int pb = blockIdx.x - 216;
    const u32x4* p2 = (const u32x4*)w2; const u32x4* po = (const u32x4*)wo;
    unsigned acc = 0;
    for (int i = pb * 512 + threadIdx.x; i < FF_ * D_ / 8; i += 40 * 512) { u32x4 v = p2[i]; acc ^= v[0] ^ v[1] ^ v[2] ^ v[3]; }
    for (int i = pb * 512 + threadIdx.x; i < D_ * D_ / 8; i += 40 * 512) { u32x4 v = po[i]; acc ^= v[0] ^ v[1] ^ v[2] ^ v[3]; }
    asm volatile("" ::"v"(acc));
    return;
  }
  int mt, nt;
  { const int xcd = blockIdx.x & 7, idx = blockIdx.x >> 3;
    const int j = (xcd & 3) * 27 + idx; mt = (xcd >> 2) * 4 + (j & 3); nt = j >> 2; }
  const int tid = threadIdx.x, lane = tid & 63, w = tid >> 6, wm = w >> 1, wn = w & 1, r = lane & 31, hh = lane >> 5;
  const u16* A = x + (size_t)mt * 256 * D_;
  const u16* B; int ldb;
  if (nt < 15) { B = wqkv + nt * 128; ldb = 3 * D_; } else { B = w1 + (nt - 15) * 128; ldb = FF_; }
  f32x16 acc[2][2]; float rsum, rsq, csum[2];
  const bool isV = (nt >= 10 && nt < 15);
  if (isV) G1n::run(lds, A, D_, B, ldb, acc, rsum, rsq, csum);
  else     G1s::run(lds, A, D_, B, ldb, acc, rsum, rsq, csum);
  float* st = (float*)(lds + 8 * EP_WAVE);
  {
    float s1 = rsum + __shfl_xor(rsum, 32), s2 = rsq + __shfl_xor(rsq, 32);
    float m = s1 * (1.f / D_), v = fmaxf(s2 * (1.f / D_) - m * m, 0.f);
    float c0 = csum[0] + __shfl_xor(csum[0], 32), c1 = csum[1] + __shfl_xor(csum[1], 32);
    if (hh == 0) {
      const int row = (wm * 2 + wn) * 32 + r;
      st[row] = m; st[256 + row] = rsqrtf(v + 1e-5f);
      st[512 + wm * 128 + wn * 64 + r] = c0; st[512 + wm * 128 + wn * 64 + 32 + r] = c1;
    }
  }
  bar();
  char* ep = lds + w * EP_WAVE;
  const int tok0 = mt * 256 + wm * 64, n0 = wn * 64;
  if (!isV) {
    u16* dst; int ldd, cbase; bool relu = false;
    if (nt < 5) { dst = qb; ldd = D_; cbase = nt * 128; }
    else if (nt < 10) { dst = kb; ldd = D_; cbase = (nt - 5) * 128; }
    else { dst = hb; ldd = FF_; cbase = (nt - 15) * 128; relu = true; }
    float mean[2], rstd[2];
#pragma unroll
    for (int mi = 0; mi < 2; ++mi) { mean[mi] = st[wm * 64 + mi * 32 + r]; rstd[mi] = st[256 + wm * 64 + mi * 32 + r]; }
#pragma unroll
    for (int ni = 0; ni < 2; ++ni) {
#pragma unroll
      for (int qd = 0; qd < 4; ++qd) {
        const float* cp = st + 512 + n0 + ni * 32 + 8 * qd + 4 * hh;
        f32x4 cs = *(const f32x4*)(cp) + *(const f32x4*)(cp + 128) + *(const f32x4*)(cp + 256) + *(const f32x4*)(cp + 384);
#pragma unroll
        for (int mi = 0; mi < 2; ++mi) {
          float v[4];
#pragma unroll
          for (int j = 0; j < 4; ++j) {
            float t = rstd[mi] * (acc[mi][ni][4 * qd + j] - mean[mi] * cs[j]);
            v[j] = relu ? fmaxf(t, 0.f) : t;
          }
          *(u32x2*)(ep + (mi * 32 + r) * EP_RS + (ni * 32 + 8 * qd + 4 * hh) * 2) = (u32x2){pk2(v[0], v[1]), pk2(v[2], v[3])};
        }
      }
    }
    wave_lds_sync();
#pragma unroll
    for (int i = 0; i < 8; ++i) {
      const int row = i * 8 + (lane >> 3), ch = lane & 7;
      u32x4 v = *(const u32x4*)(ep + row * EP_RS + ch * 16);
      *(u32x4*)(dst + (size_t)(tok0 + row) * ldd + cbase + n0 + ch * 8) = v;
    }
  } else {
    const int cbase = (nt - 10) * 128;
    float cs[2];
#pragma unroll
    for (int ni = 0; ni < 2; ++ni) {
      const float* cp = st + 512 + n0 + ni * 32 + r;
      cs[ni] = cp[0] + cp[128] + cp[256] + cp[384];
    }
#pragma unroll
    for (int mi = 0; mi < 2; ++mi) {
#pragma unroll
      for (int qd = 0; qd < 4; ++qd) {
        f32x4 mn = *(const f32x4*)(st + wm * 64 + mi * 32 + 8 * qd + 4 * hh);
        f32x4 rs = *(const f32x4*)(st + 256 + wm * 64 + mi * 32 + 8 * qd + 4 * hh);
#pragma unroll
        for (int ni = 0; ni < 2; ++ni) {
          float v[4];
#pragma unroll
          for (int j = 0; j < 4; ++j) v[j] = rs[j] * (acc[mi][ni][4 * qd + j] - mn[j] * cs[ni]);
          *(u32x2*)(ep + (ni * 32 + r) * EP_RS + (mi * 32 + 8 * qd + 4 * hh) * 2) = (u32x2){pk2(v[0], v[1]), pk2(v[2], v[3])};
        }
      }
    }
    wave_lds_sync();
#pragma unroll
    for (int i = 0; i < 8; ++i) {
      const int row = i * 8 + (lane >> 3), ch = lane & 7;
      u32x4 v = *(const u32x4*)(ep + row * EP_RS + ch * 16);
      *(u32x4*)(vT + (size_t)(cbase + n0 + row) * S_ + tok0 + ch * 8) = v;
    }
  }
}

typedef Gemm<128, 64, 2, 2, 3, 24, true, false> G2;
constexpr int G2_BLOCKS = (S_ / 128) * (D_ / 64);
constexpr int ATT_BLOCKS = NH_ * (S_ / 256);
constexpr int KS_BYTES = 512 * 64, VS_STRIDE = 1032;
constexpr int EP2_RS = 80;
static_assert(KS_BYTES + (DH_ + 1) * VS_STRIDE <= G2::LDS_BYTES, "lds");

__global__ __launch_bounds__(256, 2) void k2_attn_w2(const u16* __restrict__ qb, const u16* __restrict__ kb, const u16* __restrict__ vT,
                                                     const u16* __restrict__ hb, const u16* __restrict__ w2,
                                                     u16* __restrict__ ob, u16* __restrict__ gb) {
  __shared__ __attribute__((aligned(16))) char lds[G2::LDS_BYTES];
  const int tid = threadIdx.x, lane = tid & 63, w = __builtin_amdgcn_readfirstlane(tid >> 6), r = lane & 31, hh = lane >> 5;
  const int bid = blockIdx.x;
  if (bid < G2_BLOCKS) {
    const int xcd = bid & 7, idx = bid >> 3;
    const int mt = xcd * 2 + idx / 10, nt = idx % 10;
    f32x16 acc[2][1]; float d0, d1, d2[1];
    G2::run(lds, hb + (size_t)mt * 128 * FF_, FF_, w2 + nt * 64, D_, acc, d0, d1, d2);
    const int wm = w >> 1, wn = w & 1;
    char* ep = lds + w * (64 * EP2_RS);
#pragma unroll
    for (int mi = 0; mi < 2; ++mi)
#pragma unroll
      for (int qd = 0; qd < 4; ++qd)
        *(u32x2*)(ep + (mi * 32 + r) * EP2_RS + (8 * qd + 4 * hh) * 2) = (u32x2){pk2(acc[mi][0][4 * qd], acc[mi][0][4 * qd + 1]), pk2(acc[mi][0][4 * qd + 2], acc[mi][0][4 * qd + 3])};
    wave_lds_sync();
#pragma unroll
    for (int i = 0; i < 4; ++i) {
      const int row = i * 16 + (lane >> 2), ch = lane & 3;
      u32x4 v = *(const u32x4*)(ep + row * EP2_RS + ch * 16);
      *(u32x4*)(gb + (size_t)(mt * 128 + wm * 64 + row) * D_ + nt * 64 + wn * 32 + ch * 8) = v;
    }
    return;
  }
  const int ab = bid - G2_BLOCKS;
  const int h = ab >> 3, q0 = (ab & 7) * 256;
  const int kstart = q0 - WIN_;
  char* Ks = lds;
  char* Vs = lds + KS_BYTES;
  {
    u32x2 kr[2][5];
#pragma unroll
    for (int it = 0; it < 2; ++it) {
      const int key = kstart + tid + it * 256;
#pragma unroll
      for (int j = 0; j < 5; ++j) kr[it][j] = (u32x2){0u, 0u};
      if (key >= 0) {
        const u32x2* src = (const u32x2*)(kb + (size_t)key * D_ + h * DH_);
#pragma unroll
        for (int j = 0; j < 5; ++j) kr[it][j] = src[j];
      }
    }
    u32x4 vr[6];
#pragma unroll
    for (int it = 0; it < 6; ++it) {
      const int idx = tid + it * 256, d = idx >> 6, c = idx & 63, key0 = kstart + c * 8;
      vr[it] = (u32x4){0u, 0u, 0u, 0u};
      if (d == DH_) vr[it] = (u32x4){0x3f803f80u, 0x3f803f80u, 0x3f803f80u, 0x3f803f80u};
      else if (d < DH_ && key0 >= 0) vr[it] = *(const u32x4*)(vT + (size_t)(h * DH_ + d) * S_ + key0);
    }
#pragma unroll
    for (int it = 0; it < 2; ++it) {
      const int ki = tid + it * 256, sw = (ki >> 2) & 3;
      char* rowp = Ks + ki * 64;
      *(u32x4*)(rowp + ((0 ^ sw) << 4)) = (u32x4){kr[it][0][0], kr[it][0][1], kr[it][1][0], kr[it][1][1]};
      *(u32x4*)(rowp + ((1 ^ sw) << 4)) = (u32x4){kr[it][2][0], kr[it][2][1], kr[it][3][0], kr[it][3][1]};
      *(u32x4*)(rowp + ((2 ^ sw) << 4)) = (u32x4){kr[it][4][0], kr[it][4][1], 0u, 0u};
      *(u32x4*)(rowp + ((3 ^ sw) << 4)) = (u32x4){0u, 0u, 0u, 0u};
    }
#pragma unroll
    for (int it = 0; it < 6; ++it) {
      const int idx = tid + it * 256, d = idx >> 6, c = idx & 63;
      if (d <= DH_) {
        char* dst = Vs + d * VS_STRIDE + c * 16;
        *(u32x2*)(dst) = (u32x2){vr[it][0], vr[it][1]};
        *(u32x2*)(dst + 8) = (u32x2){vr[it][2], vr[it][3]};
      }
    }
  }
  bf16x8 qf0[2], qf1[2];
#pragma unroll
  for (int rep = 0; rep < 2; ++rep) {
    const u32x2* src = (const u32x2*)(qb + (size_t)(q0 + 32 * (w + 4 * rep) + r) * D_ + h * DH_);
    u32x2 a = src[2 * hh], b = src[2 * hh + 1], c = {0, 0};
    if (hh == 0) c = src[4];
    u32x4 f0 = {a[0], a[1], b[0], b[1]}, f1 = {c[0], c[1], 0u, 0u};
    qf0[rep] = __builtin_bit_cast(bf16x8, f0); qf1[rep] = __builtin_bit_cast(bf16x8, f1);
  }
  __syncthreads();
  const float NEG = -__builtin_inff();
  f32x16 zero;
#pragma unroll
  for (int i = 0; i < 16; ++i) zero[i] = 0.f;
  const float C2 = 0.22360679774997896f * 1.4426950408889634f;
  const bool dval = r <= DH_;
  const char* vrow = Vs + (dval ? r : 0) * VS_STRIDE;
#pragma unroll
  for (int rep = 0; rep < 2; ++rep) {
    const int wq = w + 4 * rep, qw = q0 + 32 * wq;
    const int t_lo = (qw >= WIN_) ? 0 : (WIN_ - qw) / 32;
    f32x16 sc[9];
    float mx = NEG, mx2 = NEG;
#pragma unroll
    for (int t = 0; t < 9; ++t) {
      if (t >= t_lo) {
        const int row = 32 * (wq + t) + r, sw = (row >> 2) & 3;
        bf16x8 k0 = *(const bf16x8*)(Ks + row * 64 + ((hh ^ sw) << 4));
        bf16x8 k1 = *(const bf16x8*)(Ks + row * 64 + (((2 + hh) ^ sw) << 4));
        f32x16 z = MFMA32(k0, qf0[rep], zero);
        z = MFMA32(k1, qf1[rep], z);
        if (t == 0) {
#pragma unroll
          for (int i = 0; i < 16; ++i) if ((i & 3) + 8 * (i >> 2) + 4 * hh <= r) z[i] = NEG;
        }
        if (t == 8) {
#pragma unroll
          for (int i = 0; i < 16; ++i) if ((i & 3) + 8 * (i >> 2) + 4 * hh > r) z[i] = NEG;
        }
        sc[t] = z;
#pragma unroll
        for (int i = 0; i < 16; i += 2) { mx = max2f(mx, z[i]); mx2 = max2f(mx2, z[i + 1]); }
      }
    }
    mx = fmaxf(mx, mx2);
    mx = fmaxf(mx, __shfl_xor(mx, 32));
    const float mo = -mx * C2;
    f32x16 oacc = zero;
#pragma unroll
    for (int t = 0; t < 9; ++t) {
      if (t >= t_lo) {
        float pv[16];
#pragma unroll
        for (int i = 0; i < 16; ++i) pv[i] = __builtin_amdgcn_exp2f(fmaf(sc[t][i], C2, mo));
#pragma unroll
        for (int s = 0; s < 2; ++s) {
          u32x4 pp = {pk2(pv[8 * s], pv[8 * s + 1]), pk2(pv[8 * s + 2], pv[8 * s + 3]), pk2(pv[8 * s + 4], pv[8 * s + 5]), pk2(pv[8 * s + 6], pv[8 * s + 7])};
          const int kidx = 32 * (wq + t) + 16 * s + 4 * hh;
          u32x2 v0 = *(const u32x2*)(vrow + kidx * 2);
          u32x2 v1 = *(const u32x2*)(vrow + kidx * 2 + 16);
          u32x4 vv = {v0[0], v0[1], v1[0], v1[1]};
          if (!dval) vv = (u32x4){0u, 0u, 0u, 0u};
          oacc = MFMA32(__builtin_bit_cast(bf16x8, vv), __builtin_bit_cast(bf16x8, pp), oacc);
        }
      }
    }
    const float inv = 1.f / __shfl(oacc[8], r + 32);
    u16* orow = ob + (size_t)(qw + r) * D_ + h * DH_ + 4 * hh;
#pragma unroll
    for (int qd = 0; qd < 3; ++qd) {
      if (qd < 2 || hh == 0) {
        u32x2 o = {pk2(oacc[4 * qd] * inv, oacc[4 * qd + 1] * inv), pk2(oacc[4 * qd + 2] * inv, oacc[4 * qd + 3] * inv)};
        *(u32x2*)(orow + 8 * qd) = o;
      }
    }
  }
}

typedef Gemm<64, 64, 2, 2, 4, 10, true, false> G3;

__global__ __launch_bounds__(256, 2) void k3_out(const u16* __restrict__ ob, const u16* __restrict__ wo, const u16* __restrict__ gb,
                                                 const u16* __restrict__ x, u16* __restrict__ out) {
  __shared__ __attribute__((aligned(16))) char lds[G3::LDS_BYTES];
  const int tid = threadIdx.x, lane = tid & 63, w = tid >> 6, r = lane & 31, hh = lane >> 5, wm = w >> 1, wn = w & 1;
  const int xcd = blockIdx.x & 7, idx = blockIdx.x >> 3;
  const int mt = xcd * 4 + idx / 10, nt = idx % 10;
  u32x4 gv[2], xv[2];
#pragma unroll
  for (int i = 0; i < 2; ++i) {
    const size_t off = (size_t)(mt * 64 + wm * 32 + i * 16 + (lane >> 2)) * D_ + nt * 64 + wn * 32 + (lane & 3) * 8;
    gv[i] = *(const u32x4*)(gb + off);
    xv[i] = *(const u32x4*)(x + off);
  }
  f32x16 acc[1][1]; float d0, d1, d2[1];
  G3::run(lds, ob + (size_t)mt * 64 * D_, D_, wo + nt * 64, D_, acc, d0, d1, d2);
  char* ep = lds + w * (32 * EP2_RS);
#pragma unroll
  for (int qd = 0; qd < 4; ++qd)
    *(u32x2*)(ep + r * EP2_RS + (8 * qd + 4 * hh) * 2) = (u32x2){pk2(acc[0][0][4 * qd], acc[0][0][4 * qd + 1]), pk2(acc[0][0][4 * qd + 2], acc[0][0][4 * qd + 3])};
  wave_lds_sync();
#pragma unroll
  for (int i = 0; i < 2; ++i) {
    const int row = i * 16 + (lane >> 2), ch = lane & 3;
    u32x4 sv = *(const u32x4*)(ep + row * EP2_RS + ch * 16);
    float o[8];
#pragma unroll
    for (int j = 0; j < 8; ++j) {
      const float g = bf2f((u16)(gv[i][j >> 1] >> (16 * (j & 1))));
      const float xx = bf2f((u16)(xv[i][j >> 1] >> (16 * (j & 1))));
      const float sf = bf2f((u16)(sv[j >> 1] >> (16 * (j & 1))));
      const float sg = rbf(g / (1.f + __expf(-g)));
      o[j] = xx + rbf(sf * sg);
    }
    u32x4 ov = {pk2(o[0], o[1]), pk2(o[2], o[3]), pk2(o[4], o[5]), pk2(o[6], o[7])};
    *(u32x4*)(out + (size_t)(mt * 64 + wm * 32 + row) * D_ + nt * 64 + wn * 32 + ch * 8) = ov;
  }
}

extern "C" void kernel_launch(void* const* d_in, const int* in_sizes, int n_in,
                              void* d_out, int out_size, void* d_ws, size_t ws_size,
                              hipStream_t stream) {
  const u16* x = (const u16*)d_in[0];
  const u16* wqkv = (const u16*)d_in[1];
  const u16* wo = (const u16*)d_in[2];
  const u16* w1 = (const u16*)d_in[4];
  const u16* w2 = (const u16*)d_in[5];
  u16* out = (u16*)d_out;
  char* ws = (char*)d_ws;
  const size_t SD = (size_t)S_ * D_ * 2;
  u16* qb = (u16*)(ws);
  u16* kb = (u16*)(ws + SD);
  u16* vT = (u16*)(ws + 2 * SD);
  u16* ob = (u16*)(ws + 3 * SD);
  u16* gb = (u16*)(ws + 4 * SD);
  u16* hb = (u16*)(ws + 5 * SD);
  k1_qkv_h<<<8 * 27 + 40, 512, 0, stream>>>(x, wqkv, w1, qb, kb, vT, hb, w2, wo);
  k2_attn_w2<<<G2_BLOCKS + ATT_BLOCKS, 256, 0, stream>>>(qb, kb, vT, hb, w2, ob, gb);
  k3_out<<<(S_ / 64) * (D_ / 64), 256, 0, stream>>>(ob, wo, gb, x, out);
}
```
